```python
import torch

def _softmax(x, axis=-1):
    return torch.softmax(x, dim=axis)
def _gelu(x):
    return torch.nn.functional.gelu(x, approximate="tanh")
def _silu(x):
    return torch.nn.functional.silu(x)
def _sigmoid(x):
    return torch.sigmoid(x)
def _layernorm(x, axis=-1, eps=1e-5):
    m = x.mean(dim=axis, keepdim=True)
    v = x.var(dim=axis, keepdim=True, unbiased=False)
    return (x - m) / torch.sqrt(v + eps)
def _scatter_add(tgt, idx, src, axis=0):
    return tgt.clone().index_add_(axis, idx, src)
def _segment_sum(src, seg, n):
    out = torch.zeros((n,) + src.shape[1:], dtype=src.dtype)
    return out.index_add_(0, seg, src)
def _pad(x, pw):
    flat = tuple(v for pair in reversed(pw) for v in pair)
    return torch.nn.functional.pad(x, flat)

kernel_name = 'sequential_preLN__grouped_kv_mqa__dense_activation_squared_relu__prompt'

def setup_inputs():

    _w = (lambda a: (lambda t: t.to(torch.bfloat16) if t.is_floating_point() else t)(torch.as_tensor(a)))
    def _A_setup():
        B, S, D, H = 1, 2048, 640, 32
        Dh = D // H
        return {
            "x": _w((lambda *s: torch.randn(*s, dtype=torch.bfloat16) * 0.05)(B, S, D)),
            "wq": _w((lambda *s: torch.randn(*s, dtype=torch.bfloat16) * 0.05)(D, H * Dh)),
            "wk": _w((lambda *s: torch.randn(*s, dtype=torch.bfloat16) * 0.05)(D, Dh)),
            "wv": _w((lambda *s: torch.randn(*s, dtype=torch.bfloat16) * 0.05)(D, Dh)),
            "wo": _w((lambda *s: torch.randn(*s, dtype=torch.bfloat16) * 0.05)(H * Dh, D)),
        }
    _A_in = _A_setup()

    def _B_setup():
        B, S, D, F = 1, 2048, 640, 1536
        return {
            "x": _w((lambda *s: torch.randn(*s, dtype=torch.bfloat16) * 0.05)(B, S, D)),
            "w1": _w((lambda *s: torch.randn(*s, dtype=torch.bfloat16) * 0.05)(D, F)),
            "b1": _w((lambda *s: torch.randn(*s, dtype=torch.bfloat16) * 0.8)(F)),
            "w2": _w((lambda *s: torch.randn(*s, dtype=torch.bfloat16) * 0.05)(F, D)),
            "b2": _w((lambda *s: torch.randn(*s, dtype=torch.bfloat16) * 3.2)(D)),
        }
    _B_in = _B_setup()

    return {
        "A_x": _A_in["x"],
        "A_wq": _A_in["wq"],
        "A_wk": _A_in["wk"],
        "A_wv": _A_in["wv"],
        "A_wo": _A_in["wo"],
        "B_x": _B_in["x"],
        "B_w1": _B_in["w1"],
        "B_b1": _B_in["b1"],
        "B_w2": _B_in["w2"],
        "B_b2": _B_in["b2"],
    }

def reference(A_x, A_wq, A_wk, A_wv, A_wo, B_x, B_w1, B_b1, B_w2, B_b2):
    def _A_ref(x, wq, wk, wv, wo):
        B, S, D = x.shape
        H = 32
        Dh = D // H
        q = (lambda x, *d: x.permute(*d))((x @ wq).reshape(B, S, H, Dh), 0, 2, 1, 3)
        k = x @ wk
        v = x @ wv
        scores = torch.einsum("bhqd,bkd->bhqk", q, k) * (Dh ** -0.5)
        p = _softmax(scores, axis=-1)
        o = torch.einsum("bhqk,bkd->bhqd", p, v)
        return (lambda x, *d: x.permute(*d))(o, 0, 2, 1, 3).reshape(B, S, H * Dh) @ wo

    def _B_ref(x, w1, b1, w2, b2):
        h = torch.einsum("bsd,df->bsf", x, w1) + b1
        zero = torch.zeros_like(h)
        r = torch.where(h > zero, h, zero)
        h = r * r
        out = torch.einsum("bsf,fd->bsd", h, w2) + b2
        return out

    x = A_x
    h = x + _A_ref(_layernorm(x, axis=-1), A_wq, A_wk, A_wv, A_wo)
    h2 = h + _B_ref(_layernorm(h, axis=-1), B_w1, B_b1, B_w2, B_b2)
    return h2
```

```cpp
#include <hip/hip_runtime.h>
#include <stdint.h>

namespace fb {

typedef unsigned short u16;
typedef unsigned long long u64;
typedef short bf16x8 __attribute__((ext_vector_type(8)));
typedef short bf16x4 __attribute__((ext_vector_type(4)));
typedef __bf16 bf16x2n __attribute__((ext_vector_type(2)));
typedef float f32x2 __attribute__((ext_vector_type(2)));
typedef float f32x4 __attribute__((ext_vector_type(4)));
typedef float f32x16 __attribute__((ext_vector_type(16)));
typedef unsigned u32x2 __attribute__((ext_vector_type(2)));
typedef unsigned u32x4 __attribute__((ext_vector_type(4)));
typedef __amdgpu_buffer_rsrc_t rsrc_t;
#define LAS __attribute__((address_space(3)))
#define DI __device__ __forceinline__

constexpr int S = 2048, D = 640, DH = 20, F = 1536;

constexpr unsigned WS_GEN = 0;
constexpr unsigned WS_FKV = 1024;
constexpr unsigned WS_FW = 4096;
constexpr unsigned WS_FO = 8192;
constexpr unsigned WS_FH = 12288;
constexpr unsigned WS_FH1 = 16384;
constexpr unsigned WS_KMAX = 20480;
constexpr unsigned WS_K = 32768;
constexpr unsigned WS_V = WS_K + 64 * 1536;
constexpr unsigned WS_O = 262144;
constexpr unsigned ACT_G = 4 * 80 * 256;
constexpr unsigned WS_H = WS_O + 32 * ACT_G;
constexpr unsigned WS_H1 = WS_H + 32 * ACT_G;
constexpr unsigned H1_G = 4 * 192 * 256;
constexpr unsigned WS_WO = WS_H1 + 32 * H1_G;
constexpr unsigned WS_W1 = WS_WO + 8 * 100 * 1024;
constexpr unsigned WS_W2 = WS_W1 + 8 * 240 * 1024;
constexpr unsigned WS_END = WS_W2 + 8 * 240 * 1024;
constexpr unsigned WS_PROF = WS_END;

constexpr int L_WQ = 0;
constexpr int L_WKV = 102400;
constexpr int L_QIMG = 131072;
constexpr int L_STAT = 143360;
constexpr int L_MISC = 147456;
constexpr int L_KM = L_MISC + 2048, L_UNSAFE = L_MISC + 2112, L_WFL = L_MISC + 2304  ;
constexpr int L_CS = 151552;
constexpr int L_HS = 153600;
constexpr int L_END = 163840;

constexpr float QSCALE = 0.22360679774997896f * 1.4426950408889634f;
constexpr float SH_OFF = 40.f, SH_LIM = 64.f;
constexpr unsigned SPIN_CAP = 1u << 16;

DI unsigned cvtpk(float lo, float hi) { f32x2 v = {lo, hi}; return __builtin_bit_cast(unsigned, __builtin_convertvector(v, bf16x2n)); }
DI float bfr(float x) { return __uint_as_float(cvtpk(x, x) << 16); }
DI float blo(unsigned r) { return __uint_as_float(r << 16); }
DI float bhi(unsigned r) { return __uint_as_float(r & 0xffff0000u); }
DI float dot2(unsigned a, unsigned b, float c) { return __builtin_amdgcn_fdot2_f32_bf16(__builtin_bit_cast(bf16x2n, a), __builtin_bit_cast(bf16x2n, b), c, false); }
DI f32x4 mfma16(u32x4 a, u32x4 b, f32x4 c) { return __builtin_amdgcn_mfma_f32_16x16x32_bf16(__builtin_bit_cast(bf16x8, a), __builtin_bit_cast(bf16x8, b), c, 0, 0, 0); }
DI f32x16 mfma32(u32x4 a, u32x4 b, f32x16 c) { return __builtin_amdgcn_mfma_f32_32x32x16_bf16(__builtin_bit_cast(bf16x8, a), __builtin_bit_cast(bf16x8, b), c, 0, 0, 0); }
DI f32x16 mfma32k8(u32x2 a, u32x2 b, f32x16 c) { return __builtin_amdgcn_mfma_f32_32x32x8bf16_1k(__builtin_bit_cast(bf16x4, a), __builtin_bit_cast(bf16x4, b), c, 0, 0, 0); }
DI rsrc_t mkrs(const void* p, unsigned bytes) { return __builtin_amdgcn_make_buffer_rsrc((void*)p, (short)0, (int)bytes, 0x00020000); }
template <int CTRL> DI float dpp(float x) { return __builtin_bit_cast(float, __builtin_amdgcn_mov_dpp(__builtin_bit_cast(int, x), CTRL, 0xf, 0xf, true)); }
DI float row16_max(float x) { x = fmaxf(x, dpp<0xB1>(x)); x = fmaxf(x, dpp<0x4E>(x)); x = fmaxf(x, dpp<0x141>(x)); x = fmaxf(x, dpp<0x128>(x)); return x; }
DI float sum16(float x) { auto s = __builtin_amdgcn_permlane16_swap(__float_as_uint(x), __float_as_uint(x), false, false); return __uint_as_float(s[0]) + __uint_as_float(s[1]); }
DI float sum32(float x) { auto s = __builtin_amdgcn_permlane32_swap(__float_as_uint(x), __float_as_uint(x), false, false); return __uint_as_float(s[0]) + __uint_as_float(s[1]); }
DI float max32(float x) { auto s = __builtin_amdgcn_permlane32_swap(__float_as_uint(x), __float_as_uint(x), false, false); return fmaxf(__uint_as_float(s[0]), __uint_as_float(s[1])); }

#define BL128(dst, voff, rs, soff, imm, MODS) asm volatile("buffer_load_dwordx4 %0, %1, %2, %3 offen offset:%4" MODS : "=v"(dst) : "v"(voff), "s"(rs), "s"(soff), "i"(imm) : "memory")
#define BL64(dst, voff, rs, soff, imm, MODS) asm volatile("buffer_load_dwordx2 %0, %1, %2, %3 offen offset:%4" MODS : "=v"(dst) : "v"(voff), "s"(rs), "s"(soff), "i"(imm) : "memory")
#define BL32(dst, voff, rs, soff, imm, MODS) asm volatile("buffer_load_dword %0, %1, %2, %3 offen offset:%4" MODS : "=v"(dst) : "v"(voff), "s"(rs), "s"(soff), "i"(imm) : "memory")
#define BL16(dst, voff, rs, soff, imm, MODS) asm volatile("buffer_load_ushort %0, %1, %2, %3 offen offset:%4" MODS : "=v"(dst) : "v"(voff), "s"(rs), "s"(soff), "i"(imm) : "memory")
#define BS128(src, voff, rs, soff, imm, MODS) asm volatile("buffer_store_dwordx4 %0, %1, %2, %3 offen offset:%4" MODS "\n\ts_nop 1" :: "v"(src), "v"(voff), "s"(rs), "s"(soff), "i"(imm) : "memory")
#define BS64(src, voff, rs, soff, imm, MODS) asm volatile("buffer_store_dwordx2 %0, %1, %2, %3 offen offset:%4" MODS :: "v"(src), "v"(voff), "s"(rs), "s"(soff), "i"(imm) : "memory")
#define BS32(src, voff, rs, soff, imm, MODS) asm volatile("buffer_store_dword %0, %1, %2, %3 offen offset:%4" MODS :: "v"(src), "v"(voff), "s"(rs), "s"(soff), "i"(imm) : "memory")
#define DMA(ldsaddr, voff, rs, soff, MODS) asm volatile("s_mov_b32 m0, %0\n\ts_nop 0\n\tbuffer_load_dwordx4 %1, %2, %3 offen" MODS " lds" :: "s"(ldsaddr), "v"(voff), "s"(rs), "s"(soff) : "memory")
#define SC1 " sc1"
#define WAITV(N) asm volatile("s_waitcnt vmcnt(" #N ")" ::: "memory")
#define WAITVI(N) asm volatile("s_waitcnt vmcnt(%0)" :: "i"(N) : "memory")
#define PIN(x) asm volatile("" : "+v"(x))
#define BAR() asm volatile("s_waitcnt lgkmcnt(0)\n\ts_barrier" ::: "memory")
#define SB() __builtin_amdgcn_sched_barrier(0)

#define STAMP(i) do {} while (0)

typedef LAS char* lptr;
template <typename T> DI T ldl(lptr p, int off) { return *(LAS T*)(p + off); }
template <typename T> DI void stl(lptr p, int off, T v) { *(LAS T*)(p + off) = v; }
typedef short v4i16_t __attribute__((ext_vector_type(4)));
DI u32x2 trread(lptr p, int off) { return __builtin_bit_cast(u32x2, __builtin_amdgcn_ds_read_tr16_b64_v4i16((LAS v4i16_t*)(p + off))); }
DI u32x4 cat(u32x2 a, u32x2 b) { return u32x4{a[0], a[1], b[0], b[1]}; }

template <int NF> DI void poll_flags(rsrc_t rws, unsigned base, int lane, u32x2 ep) {
    const unsigned voff = (unsigned)(lane & (NF - 1)) * 8u;
#pragma unroll 1
    for (unsigned spins = 0; spins < SPIN_CAP; ++spins) {
        u32x2 v; BL64(v, voff, rws, base, 0, SC1);
        asm volatile("s_waitcnt vmcnt(0)" : "+v"(v) :: "memory");
        if (__all(v[0] == ep[0] && v[1] == ep[1])) break;
        __builtin_amdgcn_s_sleep(1);
    }
}

struct KV { u32x4 k1[8]; u32x2 k2[8]; u32x4 v[8][2]; };

DI u32x4 packp(const f32x16& p, int s) {
    return u32x4{cvtpk(p[8 * s], p[8 * s + 1]), cvtpk(p[8 * s + 2], p[8 * s + 3]), cvtpk(p[8 * s + 4], p[8 * s + 5]), cvtpk(p[8 * s + 6], p[8 * s + 7])};
}
template <bool FIRST> DI void attn_step(KV& kv, u32x4 q1, u32x2 q2, f32x16& acc) {
    const f32x16 zero = {};
    if (FIRST) { asm volatile("s_waitcnt vmcnt(54)" : "+v"(kv.k1[0]), "+v"(kv.k2[0]) :: "memory"); }
    f32x16 sc = mfma32(kv.k1[0], q1, zero); sc = mfma32k8(kv.k2[0], q2, sc);
#pragma unroll
    for (int i = 0; i < 8; ++i) {
        f32x16 sn;
        if (i < 7) {
            if (FIRST) { asm volatile("s_waitcnt vmcnt(%4)" : "+v"(kv.k1[i + 1]), "+v"(kv.k2[i + 1]), "+v"(kv.v[i][0]), "+v"(kv.v[i][1]) : "i"(50 - 4 * i) : "memory"); }
            sn = mfma32(kv.k1[i + 1], q1, zero); sn = mfma32k8(kv.k2[i + 1], q2, sn);
        } else if (FIRST) { asm volatile("s_waitcnt vmcnt(24)" : "+v"(kv.v[7][0]), "+v"(kv.v[7][1]) :: "memory"); }
#pragma unroll
        for (int r = 0; r < 8; ++r) sc[r] = __builtin_amdgcn_exp2f(sc[r]);
        acc = mfma32(kv.v[i][0], packp(sc, 0), acc);
#pragma unroll
        for (int r = 8; r < 16; ++r) sc[r] = __builtin_amdgcn_exp2f(sc[r]);
        acc = mfma32(kv.v[i][1], packp(sc, 1), acc);
        if (i < 7) sc = sn;
    }
}

__global__ __launch_bounds__(512, 2) void fused(const u16* __restrict__ x, const u16* __restrict__ wq, const u16* __restrict__ wk, const u16* __restrict__ wv,
                                                const u16* __restrict__ wo, const u16* __restrict__ w1, const u16* __restrict__ b1, const u16* __restrict__ w2,
                                                const u16* __restrict__ b2, u16* __restrict__ out, char* __restrict__ ws) {
    __shared__ __attribute__((aligned(16))) char smem[L_END];
    const lptr lds = (lptr)smem;
    const unsigned lds0 = (unsigned)(uintptr_t)lds;
    const int tid = threadIdx.x, lane = tid & 63, w = __builtin_amdgcn_readfirstlane(tid >> 6);
    const int b = blockIdx.x, g = (b & 7) * 4 + (b >> 6), j = (b >> 3) & 7;
    const int c16 = lane & 15, g4 = lane >> 4;
    const rsrc_t rws = mkrs(ws, WS_END);
    const rsrc_t rx = mkrs(x, S * D * 2);
    const unsigned zoff = 0;
    STAMP(0);

    u32x2 ep;
    KV kv;
    unsigned rt[24];
    unsigned rt_dst;
    unsigned rt_d1, rt_d2;
    {
        const int m = w >> 1, kh = w & 1, wf = 2 * (j & 3);
        BL64(ep, zoff, rws, zoff, WS_GEN, SC1);
        u32x4 xr[10];
        {
            const unsigned voff = (unsigned)(c16 * 1280 + g4 * 16), soff = (unsigned)((64 * g + 16 * m) * 1280 + kh * 640);
#pragma unroll
            for (int s = 0; s < 10; ++s) BL128(xr[s], voff, rx, soff, 64 * s, "");
        }
        if (tid == 0) stl<unsigned>(lds, L_UNSAFE, 0u);
        if (w == wf) {
            const rsrc_t rkv = mkrs(j < 4 ? wk : wv, D * DH * 2);
            const unsigned voff = (unsigned)lane * 16u;
#pragma unroll
            for (int i = 0; i < 25; ++i) DMA(lds0 + L_WKV + 1024 * i, voff, rkv, (unsigned)(1024 * i), "");
        } else {
            const rsrc_t rq = mkrs(wq, D * D * 2);
            const int r = (w - wf - 1) & 7;
            unsigned off5[5];
#pragma unroll
            for (int i = 0; i < 5; ++i) { const int id = 64 * i + lane, pos = id / 10, c = id - 10 * pos; const int k = (pos & 19) | ((pos & 4) << 1) | ((pos & 8) >> 1); off5[i] = (unsigned)(k * 1280 + c * 16); }
#pragma unroll
            for (int q = 0; q < 3; ++q) { const int ks = min(r + 7 * q, 19);
#pragma unroll
                for (int i = 0; i < 5; ++i) DMA(lds0 + L_WQ + ks * 5120 + 1024 * i, off5[i], rq, (unsigned)(ks * 32 * 1280 + j * 160), ""); }
        }
        asm volatile("s_waitcnt vmcnt(15)" : "+v"(ep), "+v"(xr[0]), "+v"(xr[1]), "+v"(xr[2]), "+v"(xr[3]), "+v"(xr[4]), "+v"(xr[5]), "+v"(xr[6]), "+v"(xr[7]), "+v"(xr[8]), "+v"(xr[9]) :: "memory");
        STAMP(1);
        { const u64 e = (((u64)ep[1] << 32) | ep[0]) * 6364136223846793005ull + 1442695040888963407ull; ep = u32x2{(unsigned)e, (unsigned)(e >> 32)}; }
        float s1a = 0.f, s1b = 0.f, s2a = 0.f, s2b = 0.f;
#pragma unroll
        for (int s = 0; s < 10; ++s) {
            s1a = dot2(xr[s][0], 0x3f803f80u, s1a); s2a = dot2(xr[s][0], xr[s][0], s2a); s1b = dot2(xr[s][1], 0x3f803f80u, s1b); s2b = dot2(xr[s][1], xr[s][1], s2b);
            s1a = dot2(xr[s][2], 0x3f803f80u, s1a); s2a = dot2(xr[s][2], xr[s][2], s2a); s1b = dot2(xr[s][3], 0x3f803f80u, s1b); s2b = dot2(xr[s][3], xr[s][3], s2b);
        }
        stl<f32x2>(lds, L_STAT + (16 * m + c16) * 64 + (kh * 4 + g4) * 8, f32x2{s1a + s1b, s2a + s2b});
        if (w == wf) WAITV(0);
        BAR();
        {
            float s1 = 0.f, s2 = 0.f;
#pragma unroll
            for (int i = 0; i < 4; ++i) { const f32x4 v = ldl<f32x4>(lds, L_STAT + (16 * m + c16) * 64 + 16 * i); s1 += v[0] + v[2]; s2 += v[1] + v[3]; }
            const float mean = s1 * (1.f / D), var = fmaxf(s2 * (1.f / D) - mean * mean, 0.f);
            const float mb = bfr(mean), dd = bfr(__builtin_amdgcn_sqrtf(bfr(bfr(var) + 1e-5f)));
            const float ri = __builtin_amdgcn_rcpf(dd), nb = -mb * ri;
#pragma unroll
            for (int s = 0; s < 10; ++s)
#pragma unroll
                for (int i = 0; i < 4; ++i) xr[s][i] = cvtpk(fmaf(blo(xr[s][i]), ri, nb), fmaf(bhi(xr[s][i]), ri, nb));
        }
        f32x4 ka0 = {}, ka1 = {};
        if (m == (j & 3)) {
            const int base = L_WKV + (320 * kh + 8 * g4 + (c16 >> 2)) * 40 + (lane & 3) * 8;
            if (j < 4) {
#pragma unroll
                for (int s = 0; s < 10; ++s) {
                    ka0 = mfma16(cat(trread(lds, base + s * 1280), trread(lds, base + s * 1280 + 160)), xr[s], ka0);
                    ka1 = mfma16(cat(trread(lds, base + s * 1280 + 32), trread(lds, base + s * 1280 + 192)), xr[s], ka1);
                }
            } else {
#pragma unroll
                for (int s = 0; s < 10; ++s) {
                    ka0 = mfma16(xr[s], cat(trread(lds, base + s * 1280), trread(lds, base + s * 1280 + 160)), ka0);
                    ka1 = mfma16(xr[s], cat(trread(lds, base + s * 1280 + 32), trread(lds, base + s * 1280 + 192)), ka1);
                }
            }
            if (kh) { stl<f32x4>(lds, L_MISC + lane * 16, ka0); stl<f32x4>(lds, L_MISC + 1024 + lane * 16, ka1); }
        }
        BAR();
        if (w == wf) {
            ka0 += ldl<f32x4>(lds, L_MISC + lane * 16); ka1 += ldl<f32x4>(lds, L_MISC + 1024 + lane * 16);
            u32x2 p0 = {cvtpk(ka0[0], ka0[1]), cvtpk(ka0[2], ka0[3])}, p1 = {cvtpk(ka1[0], ka1[1]), cvtpk(ka1[2], ka1[3])};
            const int blk = 4 * g + m;
            if (j < 4) {
                const int r = (blk & 1) * 16 + c16;
                const unsigned soff = WS_K + (unsigned)(blk >> 1) * 1536u;
                float n2 = dot2(p0[0], p0[0], dot2(p0[1], p0[1], 0.f));
                if (g4 == 0) n2 = dot2(p1[0], p1[0], dot2(p1[1], p1[1], n2));
                n2 = row16_max(sum32(sum16(n2)));
                if (g4 == 1) p1 = u32x2{0x3f803f80u, 0u};
                const unsigned v0 = (unsigned)(((g4 >> 1) * 32 + r) * 16 + (g4 & 1) * 8);
                const unsigned v1 = g4 < 2 ? (unsigned)(1024 + (g4 * 32 + r) * 8) : 0x80000000u;
                const unsigned v2 = lane == 0 ? 0u : 0x80000000u;
                BS64(p0, v0, rws, soff, 0, SC1); BS64(p1, v1, rws, soff, 0, SC1);
                BS32(n2, v2, rws, (unsigned)(WS_KMAX + blk * 4), 0, SC1);
            } else {
                const unsigned soff = WS_V + (unsigned)blk * 672u;
                if (c16 == 4) p1 = u32x2{0x3f803f80u, 0x3f803f80u};
                const unsigned v0 = (unsigned)(((g4 & 1) * 21 + c16) * 16 + (g4 >> 1) * 8);
                const unsigned v1 = c16 < 5 ? v0 + 256u : 0x80000000u;
                BS64(p0, v0, rws, soff, 0, SC1); BS64(p1, v1, rws, soff, 0, SC1);
            }
            WAITV(0);
            const unsigned vf = lane == 0 ? 0u : 0x80000000u;
            BS64(ep, vf, rws, (unsigned)(WS_FKV + (g * 8 + j) * 8), 0, SC1);
        }
        STAMP(2);
        WAITV(0);
        BAR();
        STAMP(3);
        poll_flags<32>(rws, (unsigned)(WS_FKV + w * 256), lane, ep);
        STAMP(4);
        float km2;
        {
            const unsigned vkm = (unsigned)c16 * 4u;
            BL32(km2, vkm, rws, (unsigned)(WS_KMAX + w * 64), 0, SC1);
            const unsigned vk1 = (unsigned)lane * 16u, vk2 = (unsigned)lane * 8u;
            const int rho = lane & 31, ii = (rho & 3) + 4 * (rho >> 3), idx = ii < 10 ? 10 * ((rho >> 2) & 1) + ii : 20;
            const unsigned vv = (unsigned)(((lane >> 5) * 21 + idx) * 16);
#pragma unroll
            for (int i = 0; i < 8; ++i) {
                const unsigned sk = WS_K + (unsigned)(8 * w + i) * 1536u, sv = WS_V + (unsigned)(8 * w + i) * 1344u;
                BL128(kv.k1[i], vk1, rws, sk, 0, SC1); BL64(kv.k2[i], vk2, rws, sk, 1024, SC1);
                BL128(kv.v[i][0], vv, rws, sv, 0, SC1); BL128(kv.v[i][1], vv, rws, sv, 672, SC1);
            }
        }
        {
            const u16* src; unsigned nbytes, stride, col0; int ks, kind, nt0, nfr;
            if (g < 20) { ks = g;
                if (w < 6) { kind = 1; nt0 = 2 * w; nfr = 2; } else { kind = 0; nt0 = (w - 6) * 3; nfr = w == 6 ? 3 : 2; } }
            else { kind = 2; ks = 4 * (g - 20) + (w >> 1); nt0 = (w & 1) * 3; nfr = (w & 1) ? 2 : 3; }
            if (kind == 0) { src = wo; nbytes = D * D * 2; stride = 1280; col0 = 80 * j; rt_dst = WS_WO + (unsigned)((j * 20 + ks) * 5 + nt0) * 1024u; }
            else if (kind == 1) { src = w1; nbytes = D * F * 2; stride = 3072; col0 = 192 * j; rt_dst = WS_W1 + (unsigned)((j * 20 + ks) * 12 + nt0) * 1024u; }
            else { src = w2; nbytes = D * F * 2; stride = 1280; col0 = 80 * j; rt_dst = WS_W2 + (unsigned)((j * 48 + ks) * 5 + nt0) * 1024u; }
            const rsrc_t rsw = mkrs(src, nbytes);
            const int C = 4 * ks + g4;
            int r0, r2, r4, r6;
            if (kind == 0) { const int blk = C / 5, c = C - 5 * blk;
                if (c < 4) { r0 = 40 * blk + 20 * (c & 1) + 10 * (c >> 1); r2 = r0 + 2; r4 = r0 + 4; r6 = r0 + 6; }
                else { r0 = 40 * blk + 8; r2 = r0 + 20; r4 = r0 + 10; r6 = r0 + 30; } }
            else if (kind == 1) { const int js = C / 10, c = C - 10 * js;
                if (c < 8) { r0 = 80 * js + 32 * (c >> 2) + 4 * (c & 3); r2 = r0 + 2; r4 = r0 + 16; r6 = r0 + 18; }
                else { r0 = 80 * js + 64 + 8 * (c - 8); r2 = r0 + 2; r4 = r0 + 4; r6 = r0 + 6; } }
            else { const int blk = C / 6, c = C - 6 * blk;
                if (c < 4) { r0 = 48 * blk + 4 * c; r2 = r0 + 2; r4 = r0 + 16; r6 = r0 + 18; }
                else { r0 = 48 * blk + 32 + 8 * (c - 4); r2 = r0 + 2; r4 = r0 + 4; r6 = r0 + 6; } }
            const unsigned lc = (unsigned)c16 * 2u;
            const unsigned o0 = (unsigned)r0 * stride + lc, o2 = (unsigned)r2 * stride + lc, o4 = (unsigned)r4 * stride + lc, o6 = (unsigned)r6 * stride + lc;
            const int f2 = nfr == 3 ? 2 : 1;
            rt_d1 = rt_dst + 1024u; rt_d2 = rt_dst + (unsigned)f2 * 1024u;
#pragma unroll
            for (int f = 0; f < 3; ++f) {
                const unsigned sa = (col0 + (unsigned)(nt0 + (f == 2 ? f2 : f)) * 16u) * 2u, sb = sa + stride;
                BL16(rt[8 * f + 0], o0, rsw, sa, 0, ""); BL16(rt[8 * f + 1], o0, rsw, sb, 0, "");
                BL16(rt[8 * f + 2], o2, rsw, sa, 0, ""); BL16(rt[8 * f + 3], o2, rsw, sb, 0, "");
                BL16(rt[8 * f + 4], o4, rsw, sa, 0, ""); BL16(rt[8 * f + 5], o4, rsw, sb, 0, "");
                BL16(rt[8 * f + 6], o6, rsw, sa, 0, ""); BL16(rt[8 * f + 7], o6, rsw, sb, 0, "");
            }
        }
        f32x4 qa[5] = {};
        {
            const int base = L_WQ + kh * 51200 + (16 * (g4 >> 1) + 4 * (g4 & 1) + (c16 >> 2)) * 160 + (lane & 3) * 8;
#pragma unroll
            for (int s = 0; s < 10; ++s)
#pragma unroll
                for (int t = 0; t < 5; ++t) qa[t] = mfma16(cat(trread(lds, base + s * 5120 + t * 32), trread(lds, base + s * 5120 + t * 32 + 1280)), xr[s], qa[t]);
        }
        const int xb = L_WKV + m * 5120 + lane * 16;
        if (kh) { stl<f32x4>(lds, xb, qa[0]); stl<f32x4>(lds, xb + 1024, qa[1]); stl<f32x4>(lds, xb + 2048, qa[2]); }
        else { stl<f32x4>(lds, xb + 3072, qa[3]); stl<f32x4>(lds, xb + 4096, qa[4]); }
        asm volatile("s_waitcnt vmcnt(56)" : "+v"(km2) :: "memory");
        km2 = row16_max(km2);
        if (lane == 0) stl<float>(lds, L_KM + w * 4, km2);
        BAR();
        auto qfin = [&](f32x4 a, int t) __attribute__((always_inline)) {
            a += ldl<f32x4>(lds, xb + t * 1024);
            const int q = 4 * t + g4, hl = q / 5, dq = q - 5 * hl, u = 2 * m + (hl >> 1), r = (hl & 1) * 16 + c16;
            const int off = dq < 4 ? ((dq >> 1) * 32 + r) * 16 + (dq & 1) * 8 : 1024 + r * 8;
            stl<u32x2>(lds, L_QIMG + u * 1536 + off, u32x2{cvtpk(a[0] * QSCALE, a[1] * QSCALE), cvtpk(a[2] * QSCALE, a[3] * QSCALE)});
        };
        if (kh) { qfin(qa[3], 3); qfin(qa[4], 4); } else { qfin(qa[0], 0); qfin(qa[1], 1); qfin(qa[2], 2); }
        BAR();
    }
    STAMP(5);

    f32x16 acc = {};
    u32x4 w1f[30];
    {
        const int hi = lane >> 5;
        const int chain = L_WKV + lane * 16;
        u32x4 q1 = ldl<u32x4>(lds, L_QIMG + w * 1536 + lane * 16);
        u32x2 q2 = ldl<u32x2>(lds, L_QIMG + w * 1536 + 1024 + lane * 8);
        {
            const f32x4 ka = ldl<f32x4>(lds, L_KM), kb = ldl<f32x4>(lds, L_KM + 16);
            const float km2 = fmaxf(fmaxf(fmaxf(ka[0], ka[1]), fmaxf(ka[2], ka[3])), fmaxf(fmaxf(kb[0], kb[1]), fmaxf(kb[2], kb[3])));
            float n2 = dot2(q1[0], q1[0], dot2(q1[1], q1[1], dot2(q1[2], q1[2], dot2(q1[3], q1[3], 0.f))));
            if (!hi) n2 = dot2(q2[0], q2[0], dot2(q2[1], q2[1], n2));
            n2 = sum32(n2);
            const float bound = __builtin_amdgcn_sqrtf(n2 * km2);
            if (hi) { q2 = u32x2{cvtpk(SH_OFF - bound, 0.f), 0u}; stl<u32x2>(lds, L_QIMG + w * 1536 + 1024 + lane * 8, q2); }
            if (!(bound <= SH_LIM)) stl<unsigned>(lds, L_UNSAFE, 1u);
        }
        attn_step<true>(kv, q1, q2, acc);
        stl<f32x4>(lds, chain + w * 3072, f32x4{acc[0], acc[1], acc[2], acc[3]});
        stl<f32x4>(lds, chain + w * 3072 + 1024, f32x4{acc[4], acc[5], acc[6], acc[7]});
        stl<f32x4>(lds, chain + w * 3072 + 2048, f32x4{acc[8], acc[9], acc[10], acc[11]});
        BAR();
        if (b == 0 && tid == 0) BS64(ep, zoff, rws, zoff, WS_GEN, SC1);
        {
            asm volatile("s_waitcnt vmcnt(0)" : "+v"(rt[0]), "+v"(rt[1]), "+v"(rt[2]), "+v"(rt[3]), "+v"(rt[4]), "+v"(rt[5]), "+v"(rt[6]), "+v"(rt[7]), "+v"(rt[8]), "+v"(rt[9]), "+v"(rt[10]), "+v"(rt[11]),
                         "+v"(rt[12]), "+v"(rt[13]), "+v"(rt[14]), "+v"(rt[15]), "+v"(rt[16]), "+v"(rt[17]), "+v"(rt[18]), "+v"(rt[19]), "+v"(rt[20]), "+v"(rt[21]), "+v"(rt[22]), "+v"(rt[23]) :: "memory");
            const unsigned vo = (unsigned)lane * 16u;
            const u32x4 f0 = {rt[0] | (rt[1] << 16), rt[2] | (rt[3] << 16), rt[4] | (rt[5] << 16), rt[6] | (rt[7] << 16)};
            const u32x4 f1 = {rt[8] | (rt[9] << 16), rt[10] | (rt[11] << 16), rt[12] | (rt[13] << 16), rt[14] | (rt[15] << 16)};
            const u32x4 f2 = {rt[16] | (rt[17] << 16), rt[18] | (rt[19] << 16), rt[20] | (rt[21] << 16), rt[22] | (rt[23] << 16)};
            BS128(f0, vo, rws, rt_dst, 0, SC1); BS128(f1, vo, rws, rt_d1, 0, SC1); BS128(f2, vo, rws, rt_d2, 0, SC1);
        }
        int t0 = 1;
        if (__builtin_expect(__builtin_amdgcn_readfirstlane(ldl<unsigned>(lds, L_UNSAFE)) != 0, 0)) {
            BAR();
            const f32x16 zero = {};
            for (int u = 0; u < 8; ++u) {
                const u32x4 a1 = ldl<u32x4>(lds, L_QIMG + u * 1536 + lane * 16);
                u32x2 a2 = ldl<u32x2>(lds, L_QIMG + u * 1536 + 1024 + lane * 8);
                if (hi) a2 = u32x2{0u, 0u};
                float mx = -INFINITY;
#pragma unroll
                for (int i = 0; i < 8; ++i) {
                    f32x16 sc = mfma32(kv.k1[i], a1, zero); sc = mfma32k8(kv.k2[i], a2, sc);
#pragma unroll
                    for (int r = 0; r < 16; ++r) mx = fmaxf(mx, sc[r]);
                }
                mx = max32(mx);
                if (!hi) stl<float>(lds, L_WQ + (u * 32 + lane) * 32 + w * 4, mx);
            }
            BAR();
            {
                const f32x4 ma = ldl<f32x4>(lds, L_WQ + (w * 32 + (lane & 31)) * 32), mb = ldl<f32x4>(lds, L_WQ + (w * 32 + (lane & 31)) * 32 + 16);
                const float mx = fmaxf(fmaxf(fmaxf(ma[0], ma[1]), fmaxf(ma[2], ma[3])), fmaxf(fmaxf(mb[0], mb[1]), fmaxf(mb[2], mb[3])));
                const float mh = bfr(mx);
                if (hi) stl<u32x2>(lds, L_QIMG + w * 1536 + 1024 + lane * 8, u32x2{cvtpk(-mh, mh - mx), 0u});
                const f32x4 z4 = {};
                stl<f32x4>(lds, chain + w * 3072, z4); stl<f32x4>(lds, chain + w * 3072 + 1024, z4); stl<f32x4>(lds, chain + w * 3072 + 2048, z4);
            }
            BAR();
            t0 = 0;
        }
        auto gstep = [&](int t) __attribute__((always_inline)) {
            const int u = (w + t) & 7;
            q1 = ldl<u32x4>(lds, L_QIMG + u * 1536 + lane * 16);
            q2 = ldl<u32x2>(lds, L_QIMG + u * 1536 + 1024 + lane * 8);
            const f32x4 c0 = ldl<f32x4>(lds, chain + u * 3072), c1 = ldl<f32x4>(lds, chain + u * 3072 + 1024), c2 = ldl<f32x4>(lds, chain + u * 3072 + 2048);
            acc = f32x16{c0[0], c0[1], c0[2], c0[3], c1[0], c1[1], c1[2], c1[3], c2[0], c2[1], c2[2], c2[3], acc[12], acc[13], acc[14], acc[15]};
            attn_step<false>(kv, q1, q2, acc);
            if (t < 7) {
                stl<f32x4>(lds, chain + u * 3072, f32x4{acc[0], acc[1], acc[2], acc[3]});
                stl<f32x4>(lds, chain + u * 3072 + 1024, f32x4{acc[4], acc[5], acc[6], acc[7]});
                stl<f32x4>(lds, chain + u * 3072 + 2048, f32x4{acc[8], acc[9], acc[10], acc[11]});
            }
        };
#pragma unroll 1
        for (int t = t0; t < 5; ++t) {
            if (w == 0) { const unsigned vo = (unsigned)(lane & 15) * 16u; DMA(lds0 + L_WFL, vo, rws, (unsigned)(WS_FW + j * 256), SC1); }
            gstep(t);
            WAITV(0);
            BAR();
            if (t == t0 && tid == 0) BS64(ep, zoff, rws, (unsigned)(WS_FW + (j * 32 + g) * 8), 0, SC1);
        }
        STAMP(6);
        {
            const u32x2 fv = ldl<u32x2>(lds, L_WFL + (lane & 31) * 8);
            if (!__all(fv[0] == ep[0] && fv[1] == ep[1])) poll_flags<32>(rws, (unsigned)(WS_FW + j * 256), lane, ep);
        }
        {
            const unsigned vo = (unsigned)lane * 16u;
#pragma unroll
            for (int i = 0; i < 13; ++i) { const int p = min(w + 8 * i, 99); DMA(lds0 + L_WQ + p * 1024, vo, rws, (unsigned)(WS_WO + (j * 100 + p) * 1024), SC1); }
            const unsigned sw = WS_W1 + (unsigned)((j * 20 + 10 * (w >> 2)) * 12 + 3 * (w & 3)) * 1024u;
#pragma unroll
            for (int s = 0; s < 6; ++s) { BL128(w1f[3 * s], vo, rws, sw + s * 12288u, 0, SC1); BL128(w1f[3 * s + 1], vo, rws, sw + s * 12288u, 1024, SC1); BL128(w1f[3 * s + 2], vo, rws, sw + s * 12288u, 2048, SC1); }
        }
#pragma unroll 1
        for (int t = 5; t < 8; ++t) {
            gstep(t);
            if (t < 7) BAR();
        }
        STAMP(7);
        const int u = (w + 7) & 7;
        const float rl = __builtin_amdgcn_rcpf(acc[10]);
        const u32x4 o4 = {cvtpk(acc[0] * rl, acc[1] * rl), cvtpk(acc[2] * rl, acc[3] * rl), cvtpk(acc[4] * rl, acc[5] * rl), cvtpk(acc[6] * rl, acc[7] * rl)};
        const unsigned o1 = cvtpk(acc[8] * rl, acc[9] * rl);
        const unsigned so = WS_O + (unsigned)g * ACT_G + (unsigned)((u >> 1) * 80 + 10 * j + 5 * (u & 1)) * 256u;
        const unsigned vo = (unsigned)lane * 16u, vl = (unsigned)(c16 * 16 + g4 * 4);
        BS128(o4, vo, rws, so, 0, SC1); BS32(o1, vl, rws, so, 1024, SC1);
        asm volatile("s_waitcnt vmcnt(0)" : "+v"(w1f[0]), "+v"(w1f[1]), "+v"(w1f[2]), "+v"(w1f[3]), "+v"(w1f[4]), "+v"(w1f[5]), "+v"(w1f[6]), "+v"(w1f[7]), "+v"(w1f[8]),
                     "+v"(w1f[9]), "+v"(w1f[10]), "+v"(w1f[11]), "+v"(w1f[12]), "+v"(w1f[13]), "+v"(w1f[14]), "+v"(w1f[15]), "+v"(w1f[16]), "+v"(w1f[17]) :: "memory");
        BAR();
        if (tid == 0) BS64(ep, zoff, rws, (unsigned)(WS_FO + g * 128 + j * 8), 0, SC1);
    }
    STAMP(8);

    {
        const int m = w >> 1, kh = w & 1;
        const unsigned vo = (unsigned)lane * 16u;
        u32x2 xres[3];
        {
            const unsigned vx = (unsigned)(c16 * 1280 + g4 * 8), sx = (unsigned)((64 * g + 16 * m) * 1280 + j * 160 + kh * 64);
            BL64(xres[0], vx, rx, sx, 0, ""); BL64(xres[1], vx, rx, sx, 32, ""); BL64(xres[2], vx, rx, sx, 64, "");
        }
        if (w == 0) poll_flags<8>(rws, (unsigned)(WS_FO + g * 128), lane, ep);
        BAR();
        STAMP(9);
        u32x4 af[10];
        {
            const unsigned sa = WS_O + (unsigned)g * ACT_G + (unsigned)(m * 20 + kh * 10) * 1024u;
#pragma unroll
            for (int s = 0; s < 10; ++s) BL128(af[s], vo, rws, sa + (s >> 2) * 4096u, (s & 3) * 1024, SC1);
            const unsigned sw = WS_W1 + (unsigned)((j * 20 + 10 * (w >> 2)) * 12 + 3 * (w & 3)) * 1024u;
#pragma unroll
            for (int s = 6; s < 10; ++s) { BL128(w1f[3 * s], vo, rws, sw + s * 12288u, 0, SC1); BL128(w1f[3 * s + 1], vo, rws, sw + s * 12288u, 1024, SC1); BL128(w1f[3 * s + 2], vo, rws, sw + s * 12288u, 2048, SC1); }
        }
        f32x4 ha[5] = {};
        const int wb = L_WQ + kh * 51200 + lane * 16;
#pragma unroll
        for (int s = 0; s < 10; ++s) {
            asm volatile("s_waitcnt vmcnt(%1)" : "+v"(af[s]) : "i"(21 - s) : "memory");
#pragma unroll
            for (int t = 0; t < 5; ++t) ha[t] = mfma16(ldl<u32x4>(lds, wb + s * 5120 + t * 1024), af[s], ha[t]);
        }
        PIN(xres[0]); PIN(xres[1]); PIN(xres[2]);
        const int xb = L_WKV + m * 5120 + lane * 16;
        if (kh) { stl<f32x4>(lds, xb, ha[0]); stl<f32x4>(lds, xb + 1024, ha[1]); }
        else { stl<f32x4>(lds, xb + 2048, ha[2]); stl<f32x4>(lds, xb + 3072, ha[3]); stl<f32x4>(lds, xb + 4096, ha[4]); }
        BAR();
        auto hfin = [&](f32x4 a, int t, u32x2 xr) __attribute__((always_inline)) {
            a += ldl<f32x4>(lds, xb + t * 1024);
            const u32x2 hv = {cvtpk(a[0] + blo(xr[0]), a[1] + bhi(xr[0])), cvtpk(a[2] + blo(xr[1]), a[3] + bhi(xr[1]))};
            stl<u32x2>(lds, L_HS + (m * 5 + t) * 512 + lane * 8, hv);
            return hv;
        };
        const unsigned sh = WS_H + (unsigned)g * ACT_G + (unsigned)(m * 80 + 10 * j) * 256u;
        if (kh) {
            const u32x4 hv = cat(hfin(ha[2], 2, xres[0]), hfin(ha[3], 3, xres[1]));
            const u32x2 h4 = hfin(ha[4], 4, xres[2]);
            const unsigned v4 = (unsigned)((g4 >> 1) * 256 + c16 * 16 + (g4 & 1) * 8);
            BS128(hv, vo, rws, sh, 1024, SC1); BS64(h4, v4, rws, sh, 2048, SC1);
        } else {
            const u32x4 hv = cat(hfin(ha[0], 0, xres[0]), hfin(ha[1], 1, xres[1]));
            BS128(hv, vo, rws, sh, 0, SC1);
        }
        asm volatile("s_waitcnt vmcnt(0)" : "+v"(w1f[18]), "+v"(w1f[19]), "+v"(w1f[20]), "+v"(w1f[21]), "+v"(w1f[22]), "+v"(w1f[23]), "+v"(w1f[24]), "+v"(w1f[25]), "+v"(w1f[26]),
                     "+v"(w1f[27]), "+v"(w1f[28]), "+v"(w1f[29]) :: "memory");
        BAR();
        if (tid == 0) BS64(ep, zoff, rws, (unsigned)(WS_FH + g * 128 + j * 8), 0, SC1);
    }
    STAMP(10);

    {
        const int n4 = w & 3, kh = w >> 2;
        const unsigned vo = (unsigned)lane * 16u;
        const unsigned vo3 = kh ? 0x80000000u : vo;
        u32x4 w2f[36];
        const unsigned sw2 = WS_W2 + (unsigned)((j * 48 + 12 * n4) * 5 + 3 * kh) * 1024u;
        u32x2 bias1[3];
        {
            const rsrc_t rb1 = mkrs(b1, F * 2);
            const unsigned vb = (unsigned)g4 * 8u, sb = (unsigned)(192 * j + 48 * n4) * 2u;
            BL64(bias1[0], vb, rb1, sb, 0, ""); BL64(bias1[1], vb, rb1, sb, 32, ""); BL64(bias1[2], vb, rb1, sb, 64, "");
        }
        f32x4 cs[3] = {};
        {
            const u32x4 ones = {0x3f803f80u, 0x3f803f80u, 0x3f803f80u, 0x3f803f80u};
#pragma unroll
            for (int s = 0; s < 10; ++s)
#pragma unroll
                for (int t = 0; t < 3; ++t) cs[t] = mfma16(w1f[3 * s + t], ones, cs[t]);
            if (c16 == 0) { stl<f32x4>(lds, L_CS + w * 192 + g4 * 16, cs[0]); stl<f32x4>(lds, L_CS + w * 192 + 64 + g4 * 16, cs[1]); stl<f32x4>(lds, L_CS + w * 192 + 128 + g4 * 16, cs[2]); }
        }
        if (w == 0) poll_flags<8>(rws, (unsigned)(WS_FH + g * 128), lane, ep);
        BAR();
        STAMP(11);
        {
            const unsigned sa = WS_H + (unsigned)g * ACT_G + (unsigned)(n4 * 20 + kh * 10) * 1024u;
#pragma unroll
            for (int s = 0; s < 10; ++s) DMA(lds0 + (n4 * 20 + kh * 10 + s) * 1024, vo, rws, sa + s * 1024u, SC1);
        }
#pragma unroll
        for (int t = 0; t < 3; ++t) cs[t] += ldl<f32x4>(lds, L_CS + (w ^ 4) * 192 + t * 64 + g4 * 16);
        f32x4 za[4][3] = {};
        float s1 = 0.f, s2 = 0.f;
        int ab[4];
#pragma unroll
        for (int i = 0; i < 4; ++i) ab[i] = (((n4 + 2 * kh + i) & 3) * 20 + kh * 10) * 1024 + lane * 16;
        asm volatile("s_waitcnt vmcnt(5)" : "+v"(bias1[0]), "+v"(bias1[1]), "+v"(bias1[2]) :: "memory");
        BAR();
#pragma unroll
        for (int s = 0; s < 10; ++s) {
            if (s == 5) { WAITV(15); BAR(); }
#pragma unroll
            for (int i = 0; i < 4; ++i) {
                const u32x4 a = ldl<u32x4>(lds, ab[i] + s * 1024);
                if (i == 0) {
#pragma unroll
                    for (int e = 0; e < 4; ++e) { s1 = dot2(a[e], 0x3f803f80u, s1); s2 = dot2(a[e], a[e], s2); }
                }
#pragma unroll
                for (int t = 0; t < 3; ++t) za[i][t] = mfma16(w1f[3 * s + t], a, za[i][t]);
            }
            PIN(s1); PIN(s2);
            SB();
            BL128(w2f[3 * s], vo, rws, sw2 + s * 5120u, 0, SC1); BL128(w2f[3 * s + 1], vo, rws, sw2 + s * 5120u, 1024, SC1); BL128(w2f[3 * s + 2], vo3, rws, sw2 + s * 5120u, 2048, SC1);
        }
#pragma unroll
        for (int s = 10; s < 12; ++s) { BL128(w2f[3 * s], vo, rws, sw2 + s * 5120u, 0, SC1); BL128(w2f[3 * s + 1], vo, rws, sw2 + s * 5120u, 1024, SC1); BL128(w2f[3 * s + 2], vo3, rws, sw2 + s * 5120u, 2048, SC1); }
        STAMP(12);
        const int xw = 81920 + (w ^ 4) * 6144 + lane * 16, xr = 81920 + w * 6144 + lane * 16;
#pragma unroll
        for (int i = 0; i < 2; ++i)
#pragma unroll
            for (int t = 0; t < 3; ++t) stl<f32x4>(lds, xw + (i * 3 + t) * 1024, za[2 + i][t]);
        stl<f32x2>(lds, L_STAT + (16 * ((n4 + 2 * kh) & 3) + c16) * 64 + (kh * 4 + g4) * 8, f32x2{s1, s2});
        BAR();
#pragma unroll
        for (int i = 0; i < 2; ++i) {
            const int mt = (n4 + 2 * kh + i) & 3;
            float t1 = 0.f, t2 = 0.f;
#pragma unroll
            for (int q = 0; q < 4; ++q) { const f32x4 v = ldl<f32x4>(lds, L_STAT + (16 * mt + c16) * 64 + 16 * q); t1 += v[0] + v[2]; t2 += v[1] + v[3]; }
            const float mean = t1 * (1.f / D), var = fmaxf(t2 * (1.f / D) - mean * mean, 0.f);
            const float ri = __builtin_amdgcn_rsqf(var + 1e-5f);
            u32x2 hv[3];
#pragma unroll
            for (int t = 0; t < 3; ++t) {
                f32x4 z = za[i][t] + ldl<f32x4>(lds, xr + (i * 3 + t) * 1024);
                const float bb[4] = {blo(bias1[t][0]), bhi(bias1[t][0]), blo(bias1[t][1]), bhi(bias1[t][1])};
#pragma unroll
                for (int e = 0; e < 4; ++e) { const float zz = fmaxf(fmaf(fmaf(-mean, cs[t][e], z[e]), ri, bb[e]), 0.f); z[e] = zz * zz; }
                hv[t] = u32x2{cvtpk(z[0], z[1]), cvtpk(z[2], z[3])};
            }
            const unsigned sh = WS_H1 + (unsigned)g * H1_G + (unsigned)(mt * 192 + 24 * j + 6 * n4) * 256u;
            const unsigned v4 = (unsigned)((g4 >> 1) * 256 + c16 * 16 + (g4 & 1) * 8);
            const u32x4 h01 = cat(hv[0], hv[1]);
            BS128(h01, vo, rws, sh, 0, SC1); BS64(hv[2], v4, rws, sh, 1024, SC1);
        }
        WAITV(0);
#pragma unroll
        for (int i = 0; i < 36; ++i) PIN(w2f[i]);
        BAR();
        if (tid == 0) BS64(ep, zoff, rws, (unsigned)(WS_FH1 + g * 128 + j * 8), 0, SC1);
        STAMP(13);
        u32x2 bias2[3];
        {
            const rsrc_t rb2 = mkrs(b2, D * 2);
            const unsigned vb = (unsigned)g4 * 8u, sb = (unsigned)(80 * j + 48 * kh) * 2u;
#pragma unroll
            for (int t = 0; t < 3; ++t) BL64(bias2[t], vb, rb2, sb, 32 * t, "");
        }
        if (w == 0) poll_flags<8>(rws, (unsigned)(WS_FH1 + g * 128), lane, ep);
        BAR();
        STAMP(14);
        const unsigned sa = WS_H1 + (unsigned)g * H1_G + (unsigned)(2 * kh * 48 + 12 * n4) * 1024u;
        const unsigned ld = lds0 + (unsigned)(n4 * 4 + 2 * kh) * 1024u;
#pragma unroll
        for (int s = 0; s < 9; ++s) { DMA(ld + s * 16384u, vo, rws, sa + s * 1024u, SC1); DMA(ld + s * 16384u + 1024u, vo, rws, sa + 49152u + s * 1024u, SC1); }
        f32x4 ya[4][3] = {};
#pragma unroll
        for (int i = 0; i < 4; ++i) ab[i] = (n4 * 4 + ((n4 + i) & 3)) * 1024 + lane * 16;
#pragma unroll
        for (int s = 0; s < 12; ++s) {
            if (s == 0) { WAITV(12); BAR(); }
            if (s == 3) { WAITV(6); BAR();
#pragma unroll
                for (int q = 9; q < 12; ++q) { DMA(ld + (q - 9) * 16384u, vo, rws, sa + q * 1024u, SC1); DMA(ld + (q - 9) * 16384u + 1024u, vo, rws, sa + 49152u + q * 1024u, SC1); } }
            if (s == 6) { WAITV(6); BAR(); }
            if (s == 9) { WAITV(0); BAR(); }
            u32x4 a[4];
#pragma unroll
            for (int i = 0; i < 4; ++i) {
                a[i] = ldl<u32x4>(lds, ab[i] + (s % 9) * 16384);
                ya[i][0] = mfma16(w2f[3 * s], a[i], ya[i][0]); ya[i][1] = mfma16(w2f[3 * s + 1], a[i], ya[i][1]);
            }
            if (kh == 0) {
#pragma unroll
                for (int i = 0; i < 4; ++i) ya[i][2] = mfma16(w2f[3 * s + 2], a[i], ya[i][2]);
            }
        }
#pragma unroll
        for (int t = 0; t < 3; ++t) PIN(bias2[t]);
        STAMP(15);
        const int rb = 49152 + 3 * kh * 1024 + lane * 16;
#pragma unroll
        for (int i = 1; i < 4; ++i) {
            const int o = rb + (((n4 + i) & 3) * 3 + (i - 1)) * 5120;
            stl<f32x4>(lds, o, ya[i][0]); stl<f32x4>(lds, o + 1024, ya[i][1]);
            if (kh == 0) stl<f32x4>(lds, o + 2048, ya[i][2]);
        }
        BAR();
        const rsrc_t ro = mkrs(out, S * D * 2);
        const unsigned vout = (unsigned)(c16 * 1280 + g4 * 8), sout = (unsigned)((64 * g + 16 * n4) * 1280 + 160 * j + 96 * kh);
#pragma unroll
        for (int t = 0; t < 3; ++t) {
            if (t == 2 && kh) break;
            f32x4 y = ya[0][t];
#pragma unroll
            for (int i = 0; i < 3; ++i) y += ldl<f32x4>(lds, rb + ((n4 * 3 + i) * 5 + t) * 1024);
            const u32x2 hv = ldl<u32x2>(lds, L_HS + (n4 * 5 + 3 * kh + t) * 512 + lane * 8);
            const u32x2 o = {cvtpk(y[0] + blo(bias2[t][0]) + blo(hv[0]), y[1] + bhi(bias2[t][0]) + bhi(hv[0])), cvtpk(y[2] + blo(bias2[t][1]) + blo(hv[1]), y[3] + bhi(bias2[t][1]) + bhi(hv[1]))};
            BS64(o, vout, ro, sout, 32 * t, "");
        }
    }
    STAMP(16);
}

}

extern "C" void kernel_launch(void* const* d_in, const int* in_sizes, int n_in, void* d_out, int out_size, void* d_ws, size_t ws_size, hipStream_t stream) {
    typedef const fb::u16* cu;
    fb::fused<<<256, 512, 0, stream>>>((cu)d_in[0], (cu)d_in[1], (cu)d_in[2], (cu)d_in[3], (cu)d_in[4], (cu)d_in[6], (cu)d_in[7], (cu)d_in[8], (cu)d_in[9], (fb::u16*)d_out, (char*)d_ws);
}
```

```cpp
#include <hip/hip_runtime.h>
#include <stdint.h>

namespace fb {

typedef unsigned short u16;
typedef unsigned long long u64;
typedef short bf16x8 __attribute__((ext_vector_type(8)));
typedef short bf16x4 __attribute__((ext_vector_type(4)));
typedef __bf16 bf16x2n __attribute__((ext_vector_type(2)));
typedef float f32x2 __attribute__((ext_vector_type(2)));
typedef float f32x4 __attribute__((ext_vector_type(4)));
typedef float f32x16 __attribute__((ext_vector_type(16)));
typedef unsigned u32x2 __attribute__((ext_vector_type(2)));
typedef unsigned u32x4 __attribute__((ext_vector_type(4)));
typedef __amdgpu_buffer_rsrc_t rsrc_t;
#define LAS __attribute__((address_space(3)))
#define DI __device__ __forceinline__

constexpr int S = 2048, D = 640, DH = 20, F = 1536;

constexpr unsigned WS_GEN = 0;
constexpr unsigned WS_FKV = 1024;
constexpr unsigned WS_FW = 4096;
constexpr unsigned WS_FO = 8192;
constexpr unsigned WS_FH = 12288;
constexpr unsigned WS_FH1 = 16384;
constexpr unsigned WS_KMAX = 20480;
constexpr unsigned WS_K = 32768;
constexpr unsigned WS_V = WS_K + 64 * 1536;
constexpr unsigned WS_O = 262144;
constexpr unsigned ACT_G = 4 * 80 * 256;
constexpr unsigned WS_H = WS_O + 32 * ACT_G;
constexpr unsigned WS_H1 = WS_H + 32 * ACT_G;
constexpr unsigned H1_G = 4 * 192 * 256;
constexpr unsigned WS_WO = WS_H1 + 32 * H1_G;
constexpr unsigned WS_W1 = WS_WO + 8 * 100 * 1024;
constexpr unsigned WS_W2 = WS_W1 + 8 * 240 * 1024;
constexpr unsigned WS_END = WS_W2 + 8 * 240 * 1024;
constexpr unsigned WS_PROF = WS_END;

constexpr int L_XB = 0, L_WQ1 = 81920, L_WKV = 122880;
constexpr int L_WO = 0, L_CHAIN = 102400, L_HS = 138240;
constexpr int L_QX = 112640;
constexpr int L_QIMG = 148480;
constexpr int L_STAT = L_QIMG;
constexpr int L_CS = L_QIMG + 4096;
constexpr int L_KVX = L_QIMG + 4096;
constexpr int L_MISC = 160768;
constexpr int L_KM = L_MISC, L_UNSAFE = L_MISC + 64, L_KVF = L_MISC + 72, L_GEN = L_MISC + 128, L_WFL = L_MISC + 1024  , L_JUNK = L_MISC + 2048  ;
constexpr int L_END = 163840;

constexpr float QSCALE = 0.22360679774997896f * 1.4426950408889634f;
constexpr float SH_OFF = 40.f, SH_LIM = 64.f;
constexpr unsigned SPIN_CAP = 1u << 16;

DI unsigned cvtpk(float lo, float hi) { f32x2 v = {lo, hi}; return __builtin_bit_cast(unsigned, __builtin_convertvector(v, bf16x2n)); }
DI float bfr(float x) { return __uint_as_float(cvtpk(x, x) << 16); }
DI float blo(unsigned r) { return __uint_as_float(r << 16); }
DI float bhi(unsigned r) { return __uint_as_float(r & 0xffff0000u); }
DI float dot2(unsigned a, unsigned b, float c) { return __builtin_amdgcn_fdot2_f32_bf16(__builtin_bit_cast(bf16x2n, a), __builtin_bit_cast(bf16x2n, b), c, false); }
DI f32x4 mfma16(u32x4 a, u32x4 b, f32x4 c) { return __builtin_amdgcn_mfma_f32_16x16x32_bf16(__builtin_bit_cast(bf16x8, a), __builtin_bit_cast(bf16x8, b), c, 0, 0, 0); }
DI f32x16 mfma32(u32x4 a, u32x4 b, f32x16 c) { return __builtin_amdgcn_mfma_f32_32x32x16_bf16(__builtin_bit_cast(bf16x8, a), __builtin_bit_cast(bf16x8, b), c, 0, 0, 0); }
DI f32x16 mfma32k8(u32x2 a, u32x2 b, f32x16 c) { return __builtin_amdgcn_mfma_f32_32x32x8bf16_1k(__builtin_bit_cast(bf16x4, a), __builtin_bit_cast(bf16x4, b), c, 0, 0, 0); }
DI rsrc_t mkrs(const void* p, unsigned bytes) { return __builtin_amdgcn_make_buffer_rsrc((void*)p, (short)0, (int)bytes, 0x00020000); }
template <int CTRL> DI float dpp(float x) { return __builtin_bit_cast(float, __builtin_amdgcn_mov_dpp(__builtin_bit_cast(int, x), CTRL, 0xf, 0xf, true)); }
DI float row16_max(float x) { x = fmaxf(x, dpp<0xB1>(x)); x = fmaxf(x, dpp<0x4E>(x)); x = fmaxf(x, dpp<0x141>(x)); x = fmaxf(x, dpp<0x128>(x)); return x; }
DI float sum16(float x) { auto s = __builtin_amdgcn_permlane16_swap(__float_as_uint(x), __float_as_uint(x), false, false); return __uint_as_float(s[0]) + __uint_as_float(s[1]); }
DI float sum32(float x) { auto s = __builtin_amdgcn_permlane32_swap(__float_as_uint(x), __float_as_uint(x), false, false); return __uint_as_float(s[0]) + __uint_as_float(s[1]); }
DI float max32(float x) { auto s = __builtin_amdgcn_permlane32_swap(__float_as_uint(x), __float_as_uint(x), false, false); return fmaxf(__uint_as_float(s[0]), __uint_as_float(s[1])); }

#define BL128(dst, voff, rs, soff, imm, MODS) asm volatile("buffer_load_dwordx4 %0, %1, %2, %3 offen offset:%4" MODS : "=v"(dst) : "v"(voff), "s"(rs), "s"(soff), "i"(imm) : "memory")
#define BL64(dst, voff, rs, soff, imm, MODS) asm volatile("buffer_load_dwordx2 %0, %1, %2, %3 offen offset:%4" MODS : "=v"(dst) : "v"(voff), "s"(rs), "s"(soff), "i"(imm) : "memory")
#define BL32(dst, voff, rs, soff, imm, MODS) asm volatile("buffer_load_dword %0, %1, %2, %3 offen offset:%4" MODS : "=v"(dst) : "v"(voff), "s"(rs), "s"(soff), "i"(imm) : "memory")
#define BL16(dst, voff, rs, soff, imm, MODS) asm volatile("buffer_load_ushort %0, %1, %2, %3 offen offset:%4" MODS : "=v"(dst) : "v"(voff), "s"(rs), "s"(soff), "i"(imm) : "memory")
#define BS128(src, voff, rs, soff, imm, MODS) asm volatile("buffer_store_dwordx4 %0, %1, %2, %3 offen offset:%4" MODS "\n\ts_nop 1" :: "v"(src), "v"(voff), "s"(rs), "s"(soff), "i"(imm) : "memory")
#define BS64(src, voff, rs, soff, imm, MODS) asm volatile("buffer_store_dwordx2 %0, %1, %2, %3 offen offset:%4" MODS :: "v"(src), "v"(voff), "s"(rs), "s"(soff), "i"(imm) : "memory")
#define BS32(src, voff, rs, soff, imm, MODS) asm volatile("buffer_store_dword %0, %1, %2, %3 offen offset:%4" MODS :: "v"(src), "v"(voff), "s"(rs), "s"(soff), "i"(imm) : "memory")
#define DMA(ldsaddr, voff, rs, soff, MODS) asm volatile("s_mov_b32 m0, %0\n\ts_nop 0\n\tbuffer_load_dwordx4 %1, %2, %3 offen" MODS " lds" :: "s"(ldsaddr), "v"(voff), "s"(rs), "s"(soff) : "memory")
#define SC1 " sc1"
#define WAITV(N) asm volatile("s_waitcnt vmcnt(" #N ")" ::: "memory")
#define WAITVI(N) asm volatile("s_waitcnt vmcnt(%0)" :: "i"(N) : "memory")
#define PIN(x) asm volatile("" : "+v"(x))
#define BAR() asm volatile("s_waitcnt lgkmcnt(0)\n\ts_barrier" ::: "memory")
#define SB() __builtin_amdgcn_sched_barrier(0)

#define STAMP(i) do {} while (0)
#define CSTAMP(i) do {} while (0)

DI int fresh_lane() { int l; asm volatile("v_mbcnt_lo_u32_b32 %0, -1, 0\n\tv_mbcnt_hi_u32_b32 %0, -1, %0" : "=v"(l)); return l; }
#define LANEVARS const int lane = fresh_lane(), c16 = lane & 15, g4 = lane >> 4; const unsigned vo = (unsigned)lane * 16u, vlane0 = lane == 0 ? 0u : 0x80000000u; (void)c16; (void)g4; (void)vo; (void)vlane0
typedef LAS char* lptr;
template <typename T> DI T ldl(lptr p, int off) { return *(LAS T*)(p + off); }
template <typename T> DI void stl(lptr p, int off, T v) { *(LAS T*)(p + off) = v; }
typedef short v4i16_t __attribute__((ext_vector_type(4)));
DI u32x2 trread(lptr p, int off) { return __builtin_bit_cast(u32x2, __builtin_amdgcn_ds_read_tr16_b64_v4i16((LAS v4i16_t*)(p + off))); }
DI u32x4 cat(u32x2 a, u32x2 b) { return u32x4{a[0], a[1], b[0], b[1]}; }

template <int NF> DI void poll_flags(rsrc_t rws, unsigned base, int lane, u32x2 ep) {
    const unsigned voff = (unsigned)(lane & (NF - 1)) * 8u;
#pragma unroll 1
    for (unsigned spins = 0; spins < SPIN_CAP; ++spins) {
        u32x2 v; BL64(v, voff, rws, base, 0, SC1);
        asm volatile("s_waitcnt vmcnt(0)" : "+v"(v) :: "memory");
        if (__all(v[0] == ep[0] && v[1] == ep[1])) break;
        __builtin_amdgcn_s_sleep(1);
    }
}

constexpr int W1PRE = 12, FRONT_SLEEP = 8;
struct Att {
    u32x4 k1[8], k2[4], v[8][2];
    f32x16 s[2], acc[2];
    u32x4 q1[2], plo, phi; u32x2 q2[2];
};
#define EXP4(X, i) do { X[i] = __builtin_amdgcn_exp2f(X[i]); X[i + 1] = __builtin_amdgcn_exp2f(X[i + 1]); X[i + 2] = __builtin_amdgcn_exp2f(X[i + 2]); X[i + 3] = __builtin_amdgcn_exp2f(X[i + 3]); } while (0)
DI u32x4 packp(const f32x16& p, int s) {
    return u32x4{cvtpk(p[8 * s], p[8 * s + 1]), cvtpk(p[8 * s + 2], p[8 * s + 3]), cvtpk(p[8 * s + 4], p[8 * s + 5]), cvtpk(p[8 * s + 6], p[8 * s + 7])};
}
DI u32x2 k2half(const u32x4& k, int odd) { return odd ? u32x2{k[2], k[3]} : u32x2{k[0], k[1]}; }
DI unsigned kv_vv(int lane) { const int rho = lane & 31, ii = (rho & 3) + 4 * (rho >> 3), idx = ii < 10 ? 10 * ((rho >> 2) & 1) + ii : 20; return (unsigned)(((lane >> 5) * 21 + idx) * 16); }
template <int N> struct IC { static constexpr int value = N; };

constexpr int KV_NPRE = 10;
constexpr int kv_idx_k1(int n) { return n == 0 ? 0 : (n == 1 ? 2 : 4 + 7 * ((n - 2) >> 1) + ((n & 1) ? 4 : 0)); }
constexpr int kv_idx_kq(int n) { return kv_idx_k1(n) + ((n & 1) ? 0 : 1); }
constexpr int kv_idx_v0(int n) { return n == 7 ? 26 : kv_idx_k1(n + 2 > 8 ? 8 : n + 2) - 1; }
constexpr int kv_idx_v1(int n) { return n == 7 ? 27 : (n == 6 ? 25 : kv_idx_kq(n + 2) + 1); }
constexpr int kv_issued(int A) { return KV_NPRE + 2 * A > 28 ? 28 : KV_NPRE + 2 * A; }
static_assert(kv_idx_v0(0) == 3 && kv_idx_v1(0) == 6 && kv_idx_v0(1) == 7 && kv_idx_k1(3) == 8 && kv_idx_v1(1) == 9 && kv_idx_v0(2) == 10 && kv_idx_k1(7) == 22 && kv_idx_v1(5) == 23 && kv_idx_v0(6) == 24, "order");
template <int L> DI void kv_load(Att& a, unsigned vo, unsigned vv, rsrc_t rws, unsigned sk, unsigned sv) {
#define KVL_(n) \
    if constexpr (L == kv_idx_k1(n)) BL128(a.k1[n], vo, rws, sk + ((n) >> 1) * 3072u, ((n) & 1) * 1024, SC1); \
    if constexpr (((n) & 1) == 0 && L == kv_idx_kq(n)) BL128(a.k2[(n) >> 1], vo, rws, sk + ((n) >> 1) * 3072u, 2048, SC1); \
    if constexpr (L == kv_idx_v0(n)) BL128(a.v[n][0], vv, rws, sv + ((n) >> 1) * 2688u, ((n) & 1) * 1344, SC1); \
    if constexpr (L == kv_idx_v1(n)) BL128(a.v[n][1], vv, rws, sv + ((n) >> 1) * 2688u, ((n) & 1) * 1344 + 672, SC1);
    KVL_(0) KVL_(1) KVL_(2) KVL_(3) KVL_(4) KVL_(5) KVL_(6) KVL_(7)
#undef KVL_
}
template <int T, class FA> DI void att_tile(Att& a, FA&& act) {
    constexpr int t = T >> 3, i = T & 7, n = (i + 1) & 7, tn = ((T + 1) >> 3) & 1, p = (i + 7) & 7, tp = ((T + 15) >> 3) & 1;
    const f32x16 zero = {};
    f32x16& cur = a.s[T & 1]; f32x16& nxt = a.s[(T + 1) & 1];
    if constexpr (T < 63) {
        if constexpr (T < 7) { static_assert(kv_issued(2 * T) > kv_idx_kq(n), ""); asm volatile("s_waitcnt vmcnt(%2)" : "+v"(a.k1[n]), "+v"(a.k2[n >> 1]) : "i"(kv_issued(2 * T) - kv_idx_kq(n) - 1) : "memory"); }
        nxt = mfma32(a.k1[n], a.q1[tn], zero);
    }
    SB();
    act(IC<2 * T>{});
    EXP4(cur, 0);
    SB();
    if constexpr (T > 0) {
        if constexpr (T <= 8) { asm volatile("s_waitcnt vmcnt(%1)" : "+v"(a.v[p][1]) : "i"(kv_issued(2 * T + 1) - kv_idx_v1(p) - 1) : "memory"); }
        a.acc[tp] = mfma32(a.v[p][1], a.phi, a.acc[tp]);
    }
    SB();
    EXP4(cur, 4);
    a.plo = packp(cur, 0);
    SB();
    if constexpr (T < 63) nxt = mfma32k8(k2half(a.k2[n >> 1], n & 1), a.q2[tn], nxt);
    SB();
    act(IC<2 * T + 1>{});
    EXP4(cur, 8);
    SB();
    if constexpr (T < 8) { asm volatile("s_waitcnt vmcnt(%1)" : "+v"(a.v[i][0]) : "i"(kv_issued(2 * T + 2) - kv_idx_v0(i) - 1) : "memory"); }
    a.acc[t & 1] = mfma32(a.v[i][0], a.plo, i == 0 ? zero : a.acc[t & 1]);
    SB();
    EXP4(cur, 12);
    a.phi = packp(cur, 1);
    SB();
}
template <int T0, int T1, class FA> DI void att_run(Att& a, FA&& act) {
    if constexpr (T0 < T1) { att_tile<T0>(a, act); att_run<T0 + 1, T1>(a, act); }
}
#define DSW32(addr, val, imm) asm volatile("ds_write_b32 %0, %1 offset:%2" :: "v"(addr), "v"(val), "i"(imm) : "memory")
#define DSADD(addr, val, imm) asm volatile("ds_add_f32 %0, %1 offset:%2" :: "v"(addr), "v"(val), "i"(imm) : "memory")
#define BL128Z(dst, cond, voff, rs, soff, imm, MODS) asm volatile("s_cmp_lg_u32 %5, 0\n\ts_cbranch_scc1 1f\n\tbuffer_load_dwordx4 %0, %1, %2, %3 offen offset:%4" MODS "\n1:" : "=v"(dst) : "v"(voff), "s"(rs), "s"(soff), "i"(imm), "s"(cond) : "memory", "scc")

__global__ __launch_bounds__(512, 2) void fused(const u16* __restrict__ x, const u16* __restrict__ wq, const u16* __restrict__ wk, const u16* __restrict__ wv,
                                                const u16* __restrict__ wo, const u16* __restrict__ w1, const u16* __restrict__ b1, const u16* __restrict__ w2,
                                                const u16* __restrict__ b2, u16* __restrict__ out, char* __restrict__ ws) {
    __shared__ __attribute__((aligned(16))) char smem[L_END];
    const lptr lds = (lptr)smem;
    const unsigned lds0 = (unsigned)(uintptr_t)lds;
    const int w = __builtin_amdgcn_readfirstlane(threadIdx.x >> 6);
    const int b = blockIdx.x, g = (b & 7) * 4 + (b >> 6), j = (b >> 3) & 7;
    STAMP(0); CSTAMP(0);
    {
        u64 pc; unsigned len; LANEVARS;
        asm volatile("s_getpc_b64 %0\n.Lfb_code0:\n\ts_mov_b32 %1, .Lfb_code1-.Lfb_code0" : "=s"(pc), "=s"(len));
        if (w >= 6) { const rsrc_t rc = mkrs((const void*)pc, len); DMA(lds0 + L_JUNK, vo, rc, (unsigned)(((b >> 3) + 32 * (w - 6)) * 1024), ""); }
    }
    const rsrc_t rws = mkrs(ws, WS_END);
    const rsrc_t rx = mkrs(x, S * D * 2);
    const unsigned zoff = 0;

    u32x2 ep;
    Att at;
    const unsigned skw = WS_K + (unsigned)w * 12288u, svw = WS_V + (unsigned)w * 10752u;
    {
        LANEVARS;
        const int m = w >> 1, kh = w & 1, wf = 2 * (j & 3);
        const int rank = w == wf ? 7 : (w == wf + 1 ? 6 : ((w - wf - 2) & 7));
        const bool kvw = rank >= 6;
        if (w == 0) stl<unsigned>(lds, L_UNSAFE, 0u);
        if (kvw) {
            __builtin_amdgcn_s_setprio(2);
            asm volatile("s_mov_b64 exec, 1\n\ts_mov_b32 m0, %0\n\ts_nop 0\n\tbuffer_load_dwordx4 %1, %2, %3 offen sc1 lds\n\ts_mov_b64 exec, -1" :: "s"(lds0 + L_GEN + w * 16), "v"(vo), "s"(rws), "s"(zoff) : "memory");
            const rsrc_t rkv = mkrs(j < 4 ? wk : wv, D * DH * 2);
#pragma unroll
            for (int i = 0; i < 12; ++i) DMA(lds0 + L_WKV + kh * 12800 + 1024 * i, vo, rkv, (unsigned)(kh * 12800 + 1024 * i), "");
            if (lane < 32) DMA(lds0 + L_WKV + kh * 12800 + 12288, vo, rkv, (unsigned)(kh * 12800 + 12288), "");
        } else __builtin_amdgcn_s_sleep(FRONT_SLEEP);
        {
            const unsigned soff = (unsigned)((64 * g + 16 * m) * 1280 + kh * 640);
#pragma unroll
            for (int i = 0; i < 5; ++i) {
                const int id = 64 * i + lane, row = id / 40, c = id - 40 * row;
                const unsigned o = (unsigned)(row * 1280 + ((c ^ (row >> 1)) * 16));
                DMA(lds0 + L_XB + rank * 10240 + 1024 * i, o, rx, soff, "");
                const unsigned o2 = o ^ 64u;
                DMA(lds0 + L_XB + rank * 10240 + 5120 + 1024 * i, o2, rx, soff + 10240u, "");
            }
        }
        const rsrc_t rq = mkrs(wq, D * D * 2);
        unsigned off5[5];
#pragma unroll
        for (int i = 0; i < 5; ++i) { const int id = 64 * i + lane, pos = id / 10, c = id - 10 * pos; const int k = (pos & 19) | ((pos & 4) << 1) | ((pos & 8) >> 1); off5[i] = (unsigned)(k * 1280 + c * 16); }
        const int ksr = rank < 3 ? rank : rank + 7;
        const unsigned sqa = (unsigned)((rank == 6 ? 9 : ksr + 3) * 40960 + j * 160), sqb = (unsigned)((rank == 6 ? 19 : ksr + 6) * 40960 + j * 160);
        const unsigned lqa = lds0 + L_XB + rank * 10240;
        u32x4 xr[10];
        auto xread = [&]() __attribute__((always_inline)) {
            const int f = (c16 >> 1) & 7, be = L_XB + rank * 10240 + c16 * 640 + ((g4 ^ f) * 16), bo = L_XB + rank * 10240 + c16 * 640 + (((4 + g4) ^ f) * 16);
#pragma unroll
            for (int s = 0; s < 10; ++s) xr[s] = ldl<u32x4>(lds, ((s & 1) ? bo : be) + (s >> 1) * 128);
        };
        float s1a = 0.f, s1b = 0.f, s2a = 0.f, s2b = 0.f;
        auto stat = [&](int s) __attribute__((always_inline)) {
            s1a = dot2(xr[s][0], 0x3f803f80u, s1a); s2a = dot2(xr[s][0], xr[s][0], s2a); s1b = dot2(xr[s][1], 0x3f803f80u, s1b); s2b = dot2(xr[s][1], xr[s][1], s2b);
            s1a = dot2(xr[s][2], 0x3f803f80u, s1a); s2a = dot2(xr[s][2], xr[s][2], s2a); s1b = dot2(xr[s][3], 0x3f803f80u, s1b); s2b = dot2(xr[s][3], xr[s][3], s2b);
        };
        auto rowstat = [&](int row, float& mb, float& ri) __attribute__((always_inline)) {
            float s1 = 0.f, s2 = 0.f;
#pragma unroll
            for (int i = 0; i < 4; ++i) { const f32x4 v = ldl<f32x4>(lds, L_STAT + row * 64 + 16 * i); s1 += v[0] + v[2]; s2 += v[1] + v[3]; }
            const float mean = s1 * (1.f / D), var = fmaxf(s2 * (1.f / D) - mean * mean, 0.f);
            mb = bfr(mean); ri = __builtin_amdgcn_rcpf(bfr(__builtin_amdgcn_sqrtf(bfr(bfr(var) + 1e-5f))));
        };
        auto mkep = [&]() __attribute__((always_inline)) {
            const u32x2 gv = ldl<u32x2>(lds, L_GEN + w * 16);
            const u64 e = (((u64)(unsigned)__builtin_amdgcn_readfirstlane(gv[1]) << 32) | (unsigned)__builtin_amdgcn_readfirstlane(gv[0])) * 6364136223846793005ull + 1442695040888963407ull;
            ep = u32x2{(unsigned)e, (unsigned)(e >> 32)};
        };
        const int stato = L_STAT + (16 * m + c16) * 64 + (kh * 4 + g4) * 8;
        if (!kvw) {
#pragma unroll
            for (int i = 0; i < 5; ++i) DMA(lds0 + L_WQ1 + rank * 5120 + 1024 * i, off5[i], rq, (unsigned)(ksr * 40960 + j * 160), "");
            WAITV(5);
            STAMP(1);
            xread();
            asm volatile("s_waitcnt lgkmcnt(0)" : "+v"(xr[0]), "+v"(xr[1]), "+v"(xr[2]), "+v"(xr[3]), "+v"(xr[4]), "+v"(xr[5]), "+v"(xr[6]), "+v"(xr[7]), "+v"(xr[8]), "+v"(xr[9]) :: "memory");
#pragma unroll
            for (int i = 0; i < 5; ++i) {
                DMA(lqa + 1024 * i, off5[i], rq, sqa, "");
                stat(2 * i); stat(2 * i + 1); PIN(s1a); PIN(s1b); PIN(s2a); PIN(s2b); SB();
            }
            stl<f32x2>(lds, stato, f32x2{s1a + s1b, s2a + s2b});
        } else {
            f32x4 kc0 = {}, kc1 = {}, ka0 = {}, ka1 = {};
            const int kvbase = L_WKV + (320 * kh + 8 * g4 + (c16 >> 2)) * 40 + (lane & 3) * 8;
            u32x4 wa[10], wb[10];
            WAITV(10);
            mkep();
            const u32x4 ones = {0x3f803f80u, 0x3f803f80u, 0x3f803f80u, 0x3f803f80u};
#pragma unroll
            for (int s = 0; s < 10; ++s) { wa[s] = cat(trread(lds, kvbase + s * 1280), trread(lds, kvbase + s * 1280 + 160)); wb[s] = cat(trread(lds, kvbase + s * 1280 + 32), trread(lds, kvbase + s * 1280 + 192)); }
            if (j < 4) {
#pragma unroll
                for (int s = 0; s < 10; ++s) { kc0 = mfma16(wa[s], ones, kc0); kc1 = mfma16(wb[s], ones, kc1); }
            } else {
#pragma unroll
                for (int s = 0; s < 10; ++s) { kc0 = mfma16(ones, wa[s], kc0); kc1 = mfma16(ones, wb[s], kc1); }
            }
            WAITV(0);
            STAMP(1);
            xread();
            if (j < 4) {
#pragma unroll
                for (int s = 0; s < 10; ++s) { ka0 = mfma16(wa[s], xr[s], ka0); ka1 = mfma16(wb[s], xr[s], ka1); stat(s); }
            } else {
#pragma unroll
                for (int s = 0; s < 10; ++s) { ka0 = mfma16(xr[s], wa[s], ka0); ka1 = mfma16(xr[s], wb[s], ka1); stat(s); }
            }
            stl<f32x2>(lds, stato, f32x2{s1a + s1b, s2a + s2b});
            if (rank == 6) {
                stl<f32x4>(lds, L_KVX + lane * 16, ka0); stl<f32x4>(lds, L_KVX + 1024 + lane * 16, ka1); stl<f32x4>(lds, L_KVX + 2048 + lane * 16, kc0); stl<f32x4>(lds, L_KVX + 3072 + lane * 16, kc1);
                asm volatile("s_waitcnt lgkmcnt(0)" ::: "memory");
                *(volatile LAS u32x2*)(lds + L_KVF) = ep;
                __builtin_amdgcn_s_setprio(0);
#pragma unroll
                for (int i = 0; i < 5; ++i) DMA(lqa + 1024 * i, off5[i], rq, sqa, "");
            } else {
#pragma unroll 1
                for (unsigned spins = 0; spins < (SPIN_CAP << 4); ++spins) {
                    const u32x2 fv = *(volatile LAS u32x2*)(lds + L_KVF);
                    if (__builtin_amdgcn_readfirstlane(fv[0]) == (int)ep[0] && __builtin_amdgcn_readfirstlane(fv[1]) == (int)ep[1]) break;
                }
                asm volatile("" ::: "memory");
                float mb, ri; rowstat(16 * m + c16, mb, ri);
                ka0 += ldl<f32x4>(lds, L_KVX + lane * 16); ka1 += ldl<f32x4>(lds, L_KVX + 1024 + lane * 16);
                kc0 += ldl<f32x4>(lds, L_KVX + 2048 + lane * 16); kc1 += ldl<f32x4>(lds, L_KVX + 3072 + lane * 16);
                const int blk = 4 * g + m;
                if (j < 4) {
                    ka0 = (ka0 - mb * kc0) * ri; ka1 = (ka1 - mb * kc1) * ri;
                    u32x2 p0 = {cvtpk(ka0[0], ka0[1]), cvtpk(ka0[2], ka0[3])}, p1 = {cvtpk(ka1[0], ka1[1]), cvtpk(ka1[2], ka1[3])};
                    const int r = (blk & 1) * 16 + c16, odd = (blk >> 1) & 1;
                    const unsigned soff = WS_K + (unsigned)(blk >> 2) * 3072u;
                    const unsigned v0 = (unsigned)(odd * 1024 + ((g4 >> 1) * 32 + r) * 16 + (g4 & 1) * 8);
                    const unsigned v1 = g4 < 2 ? (unsigned)(2048 + (g4 * 32 + r) * 16 + odd * 8) : 0x80000000u;
                    BS64(p0, v0, rws, soff, 0, SC1);
                    float n2 = dot2(p0[0], p0[0], dot2(p0[1], p0[1], 0.f));
                    if (g4 == 0) n2 = dot2(p1[0], p1[0], dot2(p1[1], p1[1], n2));
                    if (g4 == 1) p1 = u32x2{0x3f803f80u, 0u};
                    BS64(p1, v1, rws, soff, 0, SC1);
                    n2 = row16_max(sum32(sum16(n2)));
                    BS32(n2, vlane0, rws, (unsigned)(WS_KMAX + blk * 4), 0, SC1);
                } else {
#pragma unroll
                    for (int e = 0; e < 4; ++e) {
                        const float me = __shfl(mb, 4 * g4 + e), re = __shfl(ri, 4 * g4 + e);
                        ka0[e] = (ka0[e] - me * kc0[e]) * re; ka1[e] = (ka1[e] - me * kc1[e]) * re;
                    }
                    u32x2 p0 = {cvtpk(ka0[0], ka0[1]), cvtpk(ka0[2], ka0[3])}, p1 = {cvtpk(ka1[0], ka1[1]), cvtpk(ka1[2], ka1[3])};
                    const unsigned soff = WS_V + (unsigned)blk * 672u;
                    if (c16 == 4) p1 = u32x2{0x3f803f80u, 0x3f803f80u};
                    const unsigned v0 = (unsigned)(((g4 & 1) * 21 + c16) * 16 + (g4 >> 1) * 8);
                    const unsigned v1 = c16 < 5 ? v0 + 256u : 0x80000000u;
                    BS64(p0, v0, rws, soff, 0, SC1); BS64(p1, v1, rws, soff, 0, SC1);
                }
                WAITV(0);
                BS64(ep, vlane0, rws, (unsigned)(WS_FKV + (g * 8 + j) * 8), 0, SC1);
                __builtin_amdgcn_s_setprio(0);
            }
        }
        STAMP(2);
        BAR();
        { const u32x2 fv = ldl<u32x2>(lds, L_KVF); ep = u32x2{(unsigned)__builtin_amdgcn_readfirstlane(fv[0]), (unsigned)__builtin_amdgcn_readfirstlane(fv[1])}; }
        {
            float mb, ri; rowstat(16 * m + c16, mb, ri);
            const float nb = -mb * ri;
#pragma unroll
            for (int s = 0; s < 10; ++s) {
                if ((s & 1) == 0 && rank < 7) DMA(lqa + 5120 + 512 * s, off5[s >> 1], rq, sqb, "");
#pragma unroll
                for (int i = 0; i < 4; ++i) xr[s][i] = cvtpk(fmaf(blo(xr[s][i]), ri, nb), fmaf(bhi(xr[s][i]), ri, nb));
                PIN(xr[s]); SB();
            }
        }
        STAMP(3);
        f32x4 qa[5] = {};
        const int qb = (16 * (g4 >> 1) + 4 * (g4 & 1) + (c16 >> 2)) * 160 + (lane & 3) * 8;
        auto qstep = [&](int base, int s) __attribute__((always_inline)) {
#pragma unroll
            for (int t = 0; t < 5; ++t) qa[t] = mfma16(cat(trread(lds, base + t * 32), trread(lds, base + t * 32 + 1280)), xr[s], qa[t]);
        };
        WAITV(5);
        BAR();
#pragma unroll
        for (int s = 0; s < 3; ++s) qstep(L_WQ1 + kh * 15360 + qb + s * 5120, s);
#pragma unroll
        for (int s = 3; s < 6; ++s) qstep(L_XB + kh * 30720 + qb + (s - 3) * 10240, s);
        poll_flags<32>(rws, (unsigned)(WS_FKV + w * 256), lane, ep);
        STAMP(4);
        float km2;
        { const unsigned vkm = (unsigned)c16 * 4u; BL32(km2, vkm, rws, (unsigned)(WS_KMAX + w * 64), 0, SC1); }
        const unsigned vv = kv_vv(lane);
        kv_load<0>(at, vo, vv, rws, skw, svw); kv_load<1>(at, vo, vv, rws, skw, svw);
        BAR();
        qstep(L_XB + kh * 30720 + qb + 5120, 6); SB();
        kv_load<2>(at, vo, vv, rws, skw, svw); kv_load<3>(at, vo, vv, rws, skw, svw);
        qstep(L_XB + kh * 30720 + qb + 10240 + 5120, 7); SB();
        kv_load<4>(at, vo, vv, rws, skw, svw); kv_load<5>(at, vo, vv, rws, skw, svw);
        qstep(L_XB + kh * 30720 + qb + 20480 + 5120, 8); SB();
        kv_load<6>(at, vo, vv, rws, skw, svw); kv_load<7>(at, vo, vv, rws, skw, svw);
        qstep(L_XB + 61440 + kh * 5120 + qb, 9); SB();
        const int xb = L_QX + m * 5120 + lane * 16;
        if (kh) { stl<f32x4>(lds, xb, qa[0]); stl<f32x4>(lds, xb + 1024, qa[1]); stl<f32x4>(lds, xb + 2048, qa[2]); }
        else { stl<f32x4>(lds, xb + 3072, qa[3]); stl<f32x4>(lds, xb + 4096, qa[4]); }
        asm volatile("s_waitcnt vmcnt(8)" : "+v"(km2) :: "memory");
        km2 = row16_max(km2);
        if (lane == 0) stl<float>(lds, L_KM + w * 4, km2);
        BAR();
        auto qfin = [&](f32x4 a, int t) __attribute__((always_inline)) {
            a += ldl<f32x4>(lds, xb + t * 1024);
            const int q = 4 * t + g4, hl = q / 5, dq = q - 5 * hl, u = 2 * m + (hl >> 1), r = (hl & 1) * 16 + c16;
            const int off = dq < 4 ? ((dq >> 1) * 32 + r) * 16 + (dq & 1) * 8 : 1024 + r * 8;
            stl<u32x2>(lds, L_QIMG + u * 1536 + off, u32x2{cvtpk(a[0] * QSCALE, a[1] * QSCALE), cvtpk(a[2] * QSCALE, a[3] * QSCALE)});
        };
        if (kh) { qfin(qa[3], 3); qfin(qa[4], 4); } else { qfin(qa[0], 0); qfin(qa[1], 1); qfin(qa[2], 2); }
        kv_load<8>(at, vo, vv, rws, skw, svw); kv_load<9>(at, vo, vv, rws, skw, svw);
        BAR();
    }
    STAMP(5); CSTAMP(1);

    u32x4 w1f[30];
    const unsigned sw1 = WS_W1 + (unsigned)((j * 20 + 10 * (w >> 2)) * 12 + 3 * (w & 3)) * 1024u;
    {
        LANEVARS;
        const int hi = lane >> 5;
        unsigned rt[24], rt_dst, rt_d2, rt_sa, rt_s2, rt_stride, ro0, ro2, ro4, ro6; rsrc_t rsw;
        {
            const u16* src; unsigned nbytes, col0; int ks, kind, nt0, nfr;
            if (g < 20) { ks = g;
                if (w < 6) { kind = 1; nt0 = 2 * w; nfr = 2; } else { kind = 0; nt0 = (w - 6) * 3; nfr = w == 6 ? 3 : 2; } }
            else { kind = 2; ks = 4 * (g - 20) + (w >> 1); nt0 = (w & 1) * 3; nfr = (w & 1) ? 2 : 3; }
            if (kind == 0) { src = wo; nbytes = D * D * 2; rt_stride = 1280; col0 = 80 * j; rt_dst = WS_WO + (unsigned)((j * 20 + ks) * 5 + nt0) * 1024u; }
            else if (kind == 1) { src = w1; nbytes = D * F * 2; rt_stride = 3072; col0 = 192 * j; rt_dst = WS_W1 + (unsigned)((j * 20 + ks) * 12 + nt0) * 1024u; }
            else { src = w2; nbytes = D * F * 2; rt_stride = 1280; col0 = 80 * j; rt_dst = WS_W2 + (unsigned)((j * 48 + ks) * 5 + nt0) * 1024u; }
            rsw = mkrs(src, nbytes);
            const int C = 4 * ks + g4;
            int r0, r2, r4, r6;
            if (kind == 0) { const int blk = C / 5, c = C - 5 * blk;
                if (c < 4) { r0 = 40 * blk + 20 * (c & 1) + 10 * (c >> 1); r2 = r0 + 2; r4 = r0 + 4; r6 = r0 + 6; }
                else { r0 = 40 * blk + 8; r2 = r0 + 20; r4 = r0 + 10; r6 = r0 + 30; } }
            else if (kind == 1) { const int js = C / 10, c = C - 10 * js;
                if (c < 8) { r0 = 80 * js + 32 * (c >> 2) + 4 * (c & 3); r2 = r0 + 2; r4 = r0 + 16; r6 = r0 + 18; }
                else { r0 = 80 * js + 64 + 8 * (c - 8); r2 = r0 + 2; r4 = r0 + 4; r6 = r0 + 6; } }
            else { const int blk = C / 6, c = C - 6 * blk;
                if (c < 4) { r0 = 48 * blk + 4 * c; r2 = r0 + 2; r4 = r0 + 16; r6 = r0 + 18; }
                else { r0 = 48 * blk + 32 + 8 * (c - 4); r2 = r0 + 2; r4 = r0 + 4; r6 = r0 + 6; } }
            const unsigned lc = (unsigned)c16 * 2u;
            ro0 = (unsigned)r0 * rt_stride + lc; ro2 = (unsigned)r2 * rt_stride + lc; ro4 = (unsigned)r4 * rt_stride + lc; ro6 = (unsigned)r6 * rt_stride + lc;
            const int f2 = nfr == 3 ? 2 : 1;
            rt_d2 = rt_dst + (unsigned)f2 * 1024u;
            rt_sa = (col0 + (unsigned)nt0 * 16u) * 2u; rt_s2 = rt_sa + (unsigned)f2 * 32u;
        }
        at.q1[0] = ldl<u32x4>(lds, L_QIMG + w * 1536 + lane * 16);
        at.q2[0] = ldl<u32x2>(lds, L_QIMG + w * 1536 + 1024 + lane * 8);
        {
            const f32x4 ka = ldl<f32x4>(lds, L_KM), kb = ldl<f32x4>(lds, L_KM + 16);
            const float km2 = fmaxf(fmaxf(fmaxf(ka[0], ka[1]), fmaxf(ka[2], ka[3])), fmaxf(fmaxf(kb[0], kb[1]), fmaxf(kb[2], kb[3])));
            const u32x4 q1 = at.q1[0];
            float n2 = dot2(q1[0], q1[0], dot2(q1[1], q1[1], dot2(q1[2], q1[2], dot2(q1[3], q1[3], 0.f))));
            if (!hi) n2 = dot2(at.q2[0][0], at.q2[0][0], dot2(at.q2[0][1], at.q2[0][1], n2));
            n2 = sum32(n2);
            const float bound = __builtin_amdgcn_sqrtf(n2 * km2);
            if (hi) { at.q2[0] = u32x2{cvtpk(SH_OFF - bound, 0.f), 0u}; stl<u32x2>(lds, L_QIMG + w * 1536 + 1024 + lane * 8, at.q2[0]); }
            if (!(bound <= SH_LIM)) stl<unsigned>(lds, L_UNSAFE, 1u);
        }
        const unsigned vtid0 = w == 0 ? vlane0 : 0x80000000u;
        const unsigned vv = kv_vv(lane);
        f32x4 chq;
        auto act = [&](auto ic) __attribute__((always_inline)) {
            constexpr int A = decltype(ic)::value, T = A >> 1, t = T >> 3, i = T & 7, h = A & 1;
            if constexpr (KV_NPRE + 2 * A < 28) { kv_load<KV_NPRE + 2 * A>(at, vo, vv, rws, skw, svw); kv_load<KV_NPRE + 2 * A + 1>(at, vo, vv, rws, skw, svw); }
            if constexpr (t >= 1 && h == 1 && i <= 3) {
                const int ca = L_CHAIN + ((w + t - 1) & 7) * 3072 + lane * 16;
                if constexpr (i >= 1) {
                    const f32x16& ac = at.acc[(t - 1) & 1];
                    f32x4 v = {ac[4 * i - 4], ac[4 * i - 3], ac[4 * i - 2], ac[4 * i - 1]};
                    if constexpr (t >= 2) v += chq;
                    stl<f32x4>(lds, ca + (i - 1) * 1024, v);
                }
                if constexpr (t >= 2 && i <= 2) chq = ldl<f32x4>(lds, ca + i * 1024);
            }
            if constexpr (i == 6 && h == 0 && t >= 1) {
                if constexpr (t == 2) WAITV(0);
                BAR();
                if constexpr (t == 2) BS64(ep, vtid0, rws, (unsigned)(WS_FW + (j * 32 + g) * 8), 0, SC1);
            }
            if constexpr (i == 6 && h == 1 && t < 7) {
                const int u = (w + t + 1) & 7;
                at.q1[(t + 1) & 1] = ldl<u32x4>(lds, L_QIMG + u * 1536 + lane * 16);
                at.q2[(t + 1) & 1] = ldl<u32x2>(lds, L_QIMG + u * 1536 + 1024 + lane * 8);
            }
            if constexpr (A >= 17 && A < 29) {
                constexpr int f = (A - 17) >> 2, e = (A - 17) & 3;
                const unsigned sa = f == 2 ? rt_s2 : rt_sa + 32u * f, o = e == 0 ? ro0 : e == 1 ? ro2 : e == 2 ? ro4 : ro6;
                BL16(rt[8 * f + 2 * e], o, rsw, sa, 0, ""); BL16(rt[8 * f + 2 * e + 1], o, rsw, sa + rt_stride, 0, "");
            }
            if constexpr (A >= 36 && A < 39) {
                constexpr int f = A - 36;
                asm volatile("s_waitcnt vmcnt(%8)" : "+v"(rt[8 * f]), "+v"(rt[8 * f + 1]), "+v"(rt[8 * f + 2]), "+v"(rt[8 * f + 3]), "+v"(rt[8 * f + 4]), "+v"(rt[8 * f + 5]), "+v"(rt[8 * f + 6]), "+v"(rt[8 * f + 7]) : "i"(16 - 7 * f) : "memory");
                const u32x4 fr = {rt[8 * f] | (rt[8 * f + 1] << 16), rt[8 * f + 2] | (rt[8 * f + 3] << 16), rt[8 * f + 4] | (rt[8 * f + 5] << 16), rt[8 * f + 6] | (rt[8 * f + 7] << 16)};
                BS128(fr, vo, rws, f == 2 ? rt_d2 : rt_dst + 1024u * f, 0, SC1);
            }
            if constexpr (A == 50) {
                const unsigned sx = (unsigned)((64 * g + 8 * w) * 1280 + 160 * j);
                const int r = lane / 10, c = lane - 10 * r, id2 = 64 + (lane & 15), r2 = id2 / 10, c2 = id2 - 10 * r2;
                const unsigned o1 = (unsigned)(r * 1280 + c * 16), o2 = (unsigned)(r2 * 1280 + c2 * 16);
                DMA(lds0 + L_HS + w * 1280, o1, rx, sx, "");
                asm volatile("s_mov_b64 exec, 0xffff\n\ts_mov_b32 m0, %0\n\ts_nop 0\n\tbuffer_load_dwordx4 %1, %2, %3 offen lds\n\ts_mov_b64 exec, -1" :: "s"(lds0 + L_HS + w * 1280 + 1024), "v"(o2), "s"(rx), "s"(sx) : "memory");
            }
            if constexpr (A >= 66 && A < 79) {
                const int p = min(w + 8 * (A - 66), 99);
                DMA(lds0 + L_WO + p * 1024, vo, rws, (unsigned)(WS_WO + (j * 100 + p) * 1024), SC1);
            }
            if constexpr (A >= 81 && A < 97 && (A & 1) == 1) {
                const int p = min(w + 8 * ((A - 81) >> 1), 59) + 60 * (g & 3);
                DMA(lds0 + 131072, vo, rws, (unsigned)(WS_W2 + (j * 240 + p) * 1024), SC1);
            }
            if constexpr (A >= 80 && A < 112 && (A & 1) == 0) { constexpr int f = (A - 80) >> 1; if constexpr (f < W1PRE) BL128(w1f[f], vo, rws, sw1 + (f / 3) * 12288u, (f % 3) * 1024, SC1); }
            if constexpr (A >= 114) {
                constexpr int f = W1PRE + (A - 114) * (30 - W1PRE) / 14, fe = W1PRE + (A - 113) * (30 - W1PRE) / 14;
                if constexpr (f < fe) BL128(w1f[f], vo, rws, sw1 + (f / 3) * 12288u, (f % 3) * 1024, SC1);
                if constexpr (f + 1 < fe) BL128(w1f[f + 1], vo, rws, sw1 + ((f + 1) / 3) * 12288u, ((f + 1) % 3) * 1024, SC1);
            }
        };
        bool fixed = false;
#pragma unroll 1
        for (;;) {
            const f32x16 zero = {};
            asm volatile("s_waitcnt vmcnt(%2)" : "+v"(at.k1[0]), "+v"(at.k2[0]) : "i"(KV_NPRE - 2) : "memory");
            at.s[0] = mfma32(at.k1[0], at.q1[0], zero); at.s[0] = mfma32k8(k2half(at.k2[0], 0), at.q2[0], at.s[0]);
            att_run<0, 6>(at, act);
            BAR();
            if (__builtin_expect(fixed || __builtin_amdgcn_readfirstlane(ldl<unsigned>(lds, L_UNSAFE)) == 0, 1)) break;
            asm volatile("s_waitcnt vmcnt(0)" : "+v"(at.k1[6]), "+v"(at.k1[7]), "+v"(at.k2[3]) :: "memory");
#pragma unroll 1
            for (int u = 0; u < 8; ++u) {
                const u32x4 a1 = ldl<u32x4>(lds, L_QIMG + u * 1536 + lane * 16);
                u32x2 a2 = ldl<u32x2>(lds, L_QIMG + u * 1536 + 1024 + lane * 8);
                if (hi) a2 = u32x2{0u, 0u};
                float mx = -INFINITY;
#pragma unroll
                for (int i = 0; i < 8; ++i) {
                    f32x16 sc = mfma32(at.k1[i], a1, zero); sc = mfma32k8(k2half(at.k2[i >> 1], i & 1), a2, sc);
#pragma unroll
                    for (int r = 0; r < 16; ++r) mx = fmaxf(mx, sc[r]);
                }
                mx = max32(mx);
                if (!hi) stl<float>(lds, (u * 32 + lane) * 32 + w * 4, mx);
            }
            BAR();
            {
                const f32x4 ma = ldl<f32x4>(lds, (w * 32 + (lane & 31)) * 32), mb = ldl<f32x4>(lds, (w * 32 + (lane & 31)) * 32 + 16);
                const float mx = fmaxf(fmaxf(fmaxf(ma[0], ma[1]), fmaxf(ma[2], ma[3])), fmaxf(fmaxf(mb[0], mb[1]), fmaxf(mb[2], mb[3])));
                const float mh = bfr(mx);
                if (hi) { at.q2[0] = u32x2{cvtpk(-mh, mh - mx), 0u}; stl<u32x2>(lds, L_QIMG + w * 1536 + 1024 + lane * 8, at.q2[0]); }
            }
            fixed = true;
        }
        att_run<6, 30>(at, act);
        { const unsigned vf = (unsigned)(lane & 15) * 16u; asm volatile("s_mov_b64 exec, 0xffff\n\ts_mov_b32 m0, %0\n\ts_nop 0\n\tbuffer_load_dwordx4 %1, %2, %3 offen sc1 lds\n\ts_mov_b64 exec, -1"
                                                                        :: "s"(lds0 + L_WFL + w * 256), "v"(vf), "s"(rws), "s"((unsigned)(WS_FW + j * 256)) : "memory"); }
        if (b == 0) BS64(ep, vtid0, rws, zoff, WS_GEN, SC1);
        att_run<30, 32>(at, act);
        STAMP(6);
        {
            WAITV(0);
            const u32x2 fv = ldl<u32x2>(lds, L_WFL + w * 256 + (lane & 31) * 8);
            att_run<32, 33>(at, act);
            if (__builtin_expect(!__all(fv[0] == ep[0] && fv[1] == ep[1]), 0)) poll_flags<32>(rws, (unsigned)(WS_FW + j * 256), lane, ep);
        }
        att_run<33, 64>(at, act);
    }
    {
        LANEVARS;
        const int u = (w + 7) & 7;
        f32x4 ch[3];
#pragma unroll
        for (int r = 0; r < 3; ++r) ch[r] = ldl<f32x4>(lds, L_CHAIN + u * 3072 + r * 1024 + lane * 16);
        f32x16 acc = mfma32(at.v[7][1], at.phi, at.acc[1]);
        STAMP(7); CSTAMP(2);
#pragma unroll
        for (int r = 0; r < 11; ++r) acc[r] += ch[r >> 2][r & 3];
        const float rl = __builtin_amdgcn_rcpf(acc[10]);
        const u32x4 o4 = {cvtpk(acc[0] * rl, acc[1] * rl), cvtpk(acc[2] * rl, acc[3] * rl), cvtpk(acc[4] * rl, acc[5] * rl), cvtpk(acc[6] * rl, acc[7] * rl)};
        const unsigned o1 = cvtpk(acc[8] * rl, acc[9] * rl);
        const unsigned so = WS_O + (unsigned)g * ACT_G + (unsigned)((u >> 1) * 80 + 10 * j + 5 * (u & 1)) * 256u;
        const unsigned vl = (unsigned)(c16 * 16 + g4 * 4);
        BS128(o4, vo, rws, so, 0, SC1); BS32(o1, vl, rws, so, 1024, SC1);
        WAITV(0);
#pragma unroll
        for (int f = 0; f < 30; ++f) PIN(w1f[f]);
        BAR();
        if (w == 0) BS64(ep, vlane0, rws, (unsigned)(WS_FO + g * 128 + j * 8), 0, SC1);
    }
    STAMP(8);

    u32x4 w2f[36];
    const unsigned sw2 = WS_W2 + (unsigned)((j * 48 + 12 * (w & 3)) * 5 + 3 * (w >> 2)) * 1024u;
    const int khf = w >> 2;
#define LDW2(s) do { BL128(w2f[3 * (s)], vo, rws, sw2 + (s) * 5120u, 0, SC1); BL128(w2f[3 * (s) + 1], vo, rws, sw2 + (s) * 5120u, 1024, SC1); BL128Z(w2f[3 * (s) + 2], khf, vo, rws, sw2 + (s) * 5120u, 2048, SC1); } while (0)
    f32x4 cs[3] = {};
    {
        LANEVARS;
        const int m = w >> 1, kh = w & 1;
        {
            const u32x4 ones = {0x3f803f80u, 0x3f803f80u, 0x3f803f80u, 0x3f803f80u};
#pragma unroll
            for (int s = 0; s < 10; ++s)
#pragma unroll
                for (int t = 0; t < 3; ++t) cs[t] = mfma16(w1f[3 * s + t], ones, cs[t]);
            if (c16 == 0) { stl<f32x4>(lds, L_CS + w * 192 + g4 * 16, cs[0]); stl<f32x4>(lds, L_CS + w * 192 + 64 + g4 * 16, cs[1]); stl<f32x4>(lds, L_CS + w * 192 + 128 + g4 * 16, cs[2]); }
        }
        const int wb = L_WO + kh * 51200 + lane * 16;
        u32x4 wf[2][5];
#pragma unroll
        for (int t = 0; t < 5; ++t) wf[0][t] = ldl<u32x4>(lds, wb + t * 1024);
        if (w == 0) poll_flags<8>(rws, (unsigned)(WS_FO + g * 128), lane, ep);
        BAR();
        STAMP(9);
        u32x4 af[10];
        {
            const unsigned sa = WS_O + (unsigned)g * ACT_G + (unsigned)(m * 20 + kh * 10) * 1024u;
#pragma unroll
            for (int s = 0; s < 10; ++s) BL128(af[s], vo, rws, sa + (s >> 2) * 4096u, (s & 3) * 1024, SC1);
        }
#pragma unroll
        for (int t = 0; t < 3; ++t) cs[t] += ldl<f32x4>(lds, L_CS + (w ^ 4) * 192 + t * 64 + g4 * 16);
        f32x4 ha[5] = {};
#pragma unroll
        for (int s = 0; s < 10; ++s) {
            if (s < 9) {
#pragma unroll
                for (int t = 0; t < 5; ++t) wf[(s + 1) & 1][t] = ldl<u32x4>(lds, wb + (s + 1) * 5120 + t * 1024);
            }
            asm volatile("s_waitcnt vmcnt(%1)" : "+v"(af[s]) : "i"(9 - s) : "memory");
#pragma unroll
            for (int t = 0; t < 5; ++t) ha[t] = mfma16(wf[s & 1][t], af[s], ha[t]);
            SB();
        }
        const int xb = L_QX + m * 5120 + lane * 16;
        if (kh) { stl<f32x4>(lds, xb, ha[0]); stl<f32x4>(lds, xb + 1024, ha[1]); }
        else { stl<f32x4>(lds, xb + 2048, ha[2]); stl<f32x4>(lds, xb + 3072, ha[3]); stl<f32x4>(lds, xb + 4096, ha[4]); }
        STAMP(17);
        BAR();
        STAMP(18);
        auto hfin = [&](f32x4 a, int t) __attribute__((always_inline)) {
            a += ldl<f32x4>(lds, xb + t * 1024);
            const int ho = L_HS + (16 * m + c16) * 160 + t * 32 + g4 * 8;
            const u32x2 xr = ldl<u32x2>(lds, ho);
            const u32x2 hv = {cvtpk(a[0] + blo(xr[0]), a[1] + bhi(xr[0])), cvtpk(a[2] + blo(xr[1]), a[3] + bhi(xr[1]))};
            stl<u32x2>(lds, ho, hv);
            return hv;
        };
        const unsigned sh = WS_H + (unsigned)g * ACT_G + (unsigned)(m * 80 + 10 * j) * 256u;
        if (kh) {
            const u32x4 hv = cat(hfin(ha[2], 2), hfin(ha[3], 3));
            const u32x2 h4 = hfin(ha[4], 4);
            const unsigned v4 = (unsigned)((g4 >> 1) * 256 + c16 * 16 + (g4 & 1) * 8);
            BS128(hv, vo, rws, sh, 1024, SC1); BS64(h4, v4, rws, sh, 2048, SC1);
        } else {
            const u32x4 hv = cat(hfin(ha[0], 0), hfin(ha[1], 1));
            BS128(hv, vo, rws, sh, 0, SC1);
        }
        STAMP(19);
        WAITV(0);
        BAR();
        if (w == 0) BS64(ep, vlane0, rws, (unsigned)(WS_FH + g * 128 + j * 8), 0, SC1);
    }
    STAMP(10);

    {
        LANEVARS;
        const int n4 = w & 3, kh = w >> 2;
        u32x2 bias1[3], bias2[3];
        {
            const rsrc_t rb1 = mkrs(b1, F * 2), rb2 = mkrs(b2, D * 2);
            const unsigned vb = (unsigned)g4 * 8u, sb = (unsigned)(192 * j + 48 * n4) * 2u, sb2 = (unsigned)(80 * j + 48 * kh) * 2u;
            BL64(bias1[0], vb, rb1, sb, 0, ""); BL64(bias1[1], vb, rb1, sb, 32, ""); BL64(bias1[2], vb, rb1, sb, 64, "");
            BL64(bias2[0], vb, rb2, sb2, 0, ""); BL64(bias2[1], vb, rb2, sb2, 32, ""); BL64(bias2[2], vb, rb2, sb2, 64, "");
        }
        LDW2(0); LDW2(1);
        if (w == 0) poll_flags<8>(rws, (unsigned)(WS_FH + g * 128), lane, ep);
        BAR();
        STAMP(11);
        {
            const unsigned sa = WS_H + (unsigned)g * ACT_G + (unsigned)(n4 * 20 + kh * 10) * 1024u;
#pragma unroll
            for (int s = 0; s < 10; ++s) DMA(lds0 + (n4 * 20 + kh * 10 + s) * 1024, vo, rws, sa + s * 1024u, SC1);
        }
        f32x4 za[4][3] = {};
        float s1 = 0.f, s2 = 0.f;
        int ab[4];
#pragma unroll
        for (int i = 0; i < 4; ++i) ab[i] = (((n4 + 2 * kh + i) & 3) * 20 + kh * 10) * 1024 + lane * 16;
        asm volatile("s_waitcnt vmcnt(8)" : "+v"(bias1[0]), "+v"(bias1[1]), "+v"(bias1[2]), "+v"(bias2[0]), "+v"(bias2[1]), "+v"(bias2[2]) :: "memory");
        BAR();
        STAMP(23);
        u32x4 a[2][4];
#pragma unroll
        for (int i = 0; i < 4; ++i) a[0][i] = ldl<u32x4>(lds, ab[i]);
#pragma unroll
        for (int s = 0; s < 10; ++s) {
            if (s == 1) { WAITV(5); BAR(); }
            if (s == 4) { WAITV(6); BAR(); }
            if (s < 9) {
#pragma unroll
                for (int i = 0; i < 4; ++i) a[(s + 1) & 1][i] = ldl<u32x4>(lds, ab[i] + (s + 1) * 1024);
            }
#pragma unroll
            for (int i = 0; i < 4; ++i) {
#pragma unroll
                for (int t = 0; t < 3; ++t) za[i][t] = mfma16(w1f[3 * s + t], a[s & 1][i], za[i][t]);
                if (i == 0) {
#pragma unroll
                    for (int e = 0; e < 4; ++e) { s1 = dot2(a[s & 1][0][e], 0x3f803f80u, s1); s2 = dot2(a[s & 1][0][e], a[s & 1][0][e], s2); }
                }
            }
            PIN(s1); PIN(s2);
            SB();
            if (s >= 1) LDW2(s + 1);
        }
        STAMP(12);
        const int xw = 81920 + (w ^ 4) * 6144 + lane * 16, xr = 81920 + w * 6144 + lane * 16;
#pragma unroll
        for (int i = 0; i < 2; ++i)
#pragma unroll
            for (int t = 0; t < 3; ++t) stl<f32x4>(lds, xw + (i * 3 + t) * 1024, za[2 + i][t]);
        stl<f32x2>(lds, L_STAT + (16 * ((n4 + 2 * kh) & 3) + c16) * 64 + (kh * 4 + g4) * 8, f32x2{s1, s2});
        BAR();
        STAMP(20);
#pragma unroll
        for (int i = 0; i < 2; ++i) {
            const int mt = (n4 + 2 * kh + i) & 3;
            float t1 = 0.f, t2 = 0.f;
#pragma unroll
            for (int q = 0; q < 4; ++q) { const f32x4 v = ldl<f32x4>(lds, L_STAT + (16 * mt + c16) * 64 + 16 * q); t1 += v[0] + v[2]; t2 += v[1] + v[3]; }
            const float mean = t1 * (1.f / D), var = fmaxf(t2 * (1.f / D) - mean * mean, 0.f);
            const float ri = __builtin_amdgcn_rsqf(var + 1e-5f);
            u32x2 hv[3];
#pragma unroll
            for (int t = 0; t < 3; ++t) {
                f32x4 z = za[i][t] + ldl<f32x4>(lds, xr + (i * 3 + t) * 1024);
                const float bb[4] = {blo(bias1[t][0]), bhi(bias1[t][0]), blo(bias1[t][1]), bhi(bias1[t][1])};
#pragma unroll
                for (int e = 0; e < 4; ++e) { const float zz = fmaxf(fmaf(fmaf(-mean, cs[t][e], z[e]), ri, bb[e]), 0.f); z[e] = zz * zz; }
                hv[t] = u32x2{cvtpk(z[0], z[1]), cvtpk(z[2], z[3])};
            }
            const unsigned sh = WS_H1 + (unsigned)g * H1_G + (unsigned)(mt * 192 + 24 * j + 6 * n4) * 256u;
            const unsigned v4 = (unsigned)((g4 >> 1) * 256 + c16 * 16 + (g4 & 1) * 8);
            const u32x4 h01 = cat(hv[0], hv[1]);
            BS128(h01, vo, rws, sh, 0, SC1); BS64(hv[2], v4, rws, sh, 1024, SC1);
        }
        STAMP(21);
        WAITV(0);
        BAR();
        if (w == 0) BS64(ep, vlane0, rws, (unsigned)(WS_FH1 + g * 128 + j * 8), 0, SC1);
        STAMP(13);
        LDW2(11);
        if (w == 0) poll_flags<8>(rws, (unsigned)(WS_FH1 + g * 128), lane, ep);
        BAR();
        STAMP(14);
        const unsigned sa = WS_H1 + (unsigned)g * H1_G + (unsigned)(2 * kh * 48 + 12 * n4) * 1024u;
        const unsigned ld = lds0 + (unsigned)(n4 * 4 + 2 * kh) * 1024u;
#define STG(q) do { DMA(ld + ((q) & 7) * 16384u, vo, rws, sa + (q) * 1024u, SC1); DMA(ld + ((q) & 7) * 16384u + 1024u, vo, rws, sa + 49152u + (q) * 1024u, SC1); } while (0)
#pragma unroll
        for (int s = 0; s < 8; ++s) STG(s);
        f32x4 ya[4][3] = {};
#pragma unroll
        for (int i = 0; i < 4; ++i) ab[i] = (n4 * 4 + ((n4 + i) & 3)) * 1024 + lane * 16;
        WAITV(14);
#pragma unroll
        for (int i = 0; i < 36; ++i) PIN(w2f[i]);
        BAR();
        STAMP(22);
#pragma unroll
        for (int i = 0; i < 4; ++i) a[0][i] = ldl<u32x4>(lds, ab[i]);
#pragma unroll
        for (int s = 0; s < 12; ++s) {
            if (s == 0) { WAITV(10); BAR(); }
            if (s == 2) { WAITV(6); BAR(); STG(8); STG(9); }
            if (s == 4) { WAITV(6); BAR(); STG(10); STG(11); }
            if (s == 6) { WAITV(6); BAR(); }
            if (s == 8) { WAITV(2); BAR(); }
            if (s == 10) { WAITV(0); BAR(); }
            if (s < 11) {
#pragma unroll
                for (int i = 0; i < 4; ++i) a[(s + 1) & 1][i] = ldl<u32x4>(lds, ab[i] + ((s + 1) & 7) * 16384);
            }
#pragma unroll
            for (int i = 0; i < 4; ++i) { ya[i][0] = mfma16(w2f[3 * s], a[s & 1][i], ya[i][0]); ya[i][1] = mfma16(w2f[3 * s + 1], a[s & 1][i], ya[i][1]); }
            if (kh == 0) {
#pragma unroll
                for (int i = 0; i < 4; ++i) ya[i][2] = mfma16(w2f[3 * s + 2], a[s & 1][i], ya[i][2]);
            }
            SB();
        }
        STAMP(15);
        const int rb = 65536 + 3 * kh * 1024 + lane * 16;
#pragma unroll
        for (int i = 1; i < 4; ++i) {
            const int o = rb + (((n4 + i) & 3) * 3 + (i - 1)) * 5120;
            stl<f32x4>(lds, o, ya[i][0]); stl<f32x4>(lds, o + 1024, ya[i][1]);
            if (kh == 0) stl<f32x4>(lds, o + 2048, ya[i][2]);
        }
        BAR();
#pragma unroll
        for (int t = 0; t < 3; ++t) {
            if (t == 2 && kh) break;
            f32x4 y = ya[0][t];
#pragma unroll
            for (int i = 0; i < 3; ++i) y += ldl<f32x4>(lds, rb + ((n4 * 3 + i) * 5 + t) * 1024);
            const int ho = L_HS + (16 * n4 + c16) * 160 + (3 * kh + t) * 32 + g4 * 8;
            const u32x2 hv = ldl<u32x2>(lds, ho);
            stl<u32x2>(lds, ho, u32x2{cvtpk(y[0] + blo(bias2[t][0]) + blo(hv[0]), y[1] + bhi(bias2[t][0]) + bhi(hv[0])), cvtpk(y[2] + blo(bias2[t][1]) + blo(hv[1]), y[3] + bhi(bias2[t][1]) + bhi(hv[1]))});
        }
        const rsrc_t ro = mkrs(out, S * D * 2);
        const unsigned sout = (unsigned)((64 * g + 16 * n4) * 1280 + 160 * j);
        {
            const int r = lane >> 2, c = (lane & 3) * 16 + kh * 96;
            const u32x4 o = ldl<u32x4>(lds, L_HS + (16 * n4 + r) * 160 + c);
            const unsigned vout = (unsigned)(r * 1280 + c);
            BS128(o, vout, ro, sout, 0, "");
            if (kh == 0) {
                const int r2 = (lane >> 1) & 15, c2 = 64 + (lane & 1) * 16;
                const u32x4 o2 = ldl<u32x4>(lds, L_HS + (16 * n4 + r2) * 160 + c2);
                const unsigned vout2 = lane < 32 ? (unsigned)(r2 * 1280 + c2) : 0x80000000u;
                BS128(o2, vout2, ro, sout, 0, "");
            }
        }
    }
    STAMP(16); CSTAMP(3);
    asm volatile(".Lfb_code1:");
}

}

extern "C" void kernel_launch(void* const* d_in, const int* in_sizes, int n_in, void* d_out, int out_size, void* d_ws, size_t ws_size, hipStream_t stream) {
    typedef const fb::u16* cu;
    fb::fused<<<256, 512, 0, stream>>>((cu)d_in[0], (cu)d_in[1], (cu)d_in[2], (cu)d_in[3], (cu)d_in[4], (cu)d_in[6], (cu)d_in[7], (cu)d_in[8], (cu)d_in[9], (fb::u16*)d_out, (char*)d_ws);
}
```
